```python
import torch

def _softmax(x, axis=-1):
    return torch.softmax(x, dim=axis)

def _gelu(x):
    return torch.nn.functional.gelu(x, approximate='tanh')

def _silu(x):
    return torch.nn.functional.silu(x)

def _sigmoid(x):
    return torch.sigmoid(x)

def _layernorm(x, axis=-1, eps=1e-05):
    m = x.mean(dim=axis, keepdim=True)
    v = x.var(dim=axis, keepdim=True, unbiased=False)
    return (x - m) / torch.sqrt(v + eps)

def _scatter_add(tgt, idx, src, axis=0):
    return tgt.clone().index_add_(axis, idx, src)

def _segment_sum(src, seg, n):
    out = torch.zeros((n,) + src.shape[1:], dtype=src.dtype)
    return out.index_add_(0, seg, src)

def _pad(x, pw):
    flat = tuple((v for pair in reversed(pw) for v in pair))
    return torch.nn.functional.pad(x, flat)
kernel_name = 'adaLN_cond__ssm_conv_s4__glu_gated_bilinear__prompt'

def setup_inputs():

    _w = lambda a: (lambda t: t.to(torch.bfloat16) if t.is_floating_point() else t)(torch.as_tensor(a))

    def _A_setup():
        B, L, D, N = (1, 2048, 576, 128)
        A_re = -(lambda _x: torch.exp(torch.as_tensor(_x)))((lambda *s: torch.randn(*s, dtype=torch.bfloat16) * 3.2)(D, N))
        A_im = torch.pi * (lambda *s: torch.randn(*s, dtype=torch.bfloat16) * 0.05)(D, N)
        B_re = (lambda *s: torch.randn(*s, dtype=torch.bfloat16) * 0.1)(D, N)
        B_im = (lambda *s: torch.randn(*s, dtype=torch.bfloat16) * 0.1)(D, N)
        C_re = (lambda *s: torch.randn(*s, dtype=torch.bfloat16) * 0.1)(D, N)
        C_im = (lambda *s: torch.randn(*s, dtype=torch.bfloat16) * 0.1)(D, N)
        log_dt = (lambda *s: torch.randn(*s, dtype=torch.bfloat16) * 0.05)(D) * 0.5 - 4.0
        Dskip = (lambda *s: torch.randn(*s, dtype=torch.bfloat16) * 0.05)(D)
        return {'u': _w((lambda *s: torch.randn(*s, dtype=torch.bfloat16) * 0.05)(B, L, D)), 'A_re': _w(A_re), 'A_im': _w(A_im), 'B_re': _w(B_re), 'B_im': _w(B_im), 'C_re': _w(C_re), 'C_im': _w(C_im), 'log_dt': _w(log_dt), 'Dskip': _w(Dskip)}
    _A_in = _A_setup()

    def _B_setup():
        B, S, D, F = (1, 2048, 576, 4608)
        return {'x': _w((lambda *s: torch.randn(*s, dtype=torch.bfloat16) * 0.05)(B, S, D)), 'w_gate': _w((lambda *s: torch.randn(*s, dtype=torch.bfloat16) * 0.8)(D, F) * D ** (-0.5)), 'w_up': _w((lambda *s: torch.randn(*s, dtype=torch.bfloat16) * 0.8)(D, F) * D ** (-0.5)), 'w_down': _w((lambda *s: torch.randn(*s, dtype=torch.bfloat16) * 0.8)(F, D) * F ** (-0.5))}
    _B_in = _B_setup()
    return {'A_u': _A_in['u'], 'A_A_re': _A_in['A_re'], 'A_A_im': _A_in['A_im'], 'A_B_re': _A_in['B_re'], 'A_B_im': _A_in['B_im'], 'A_C_re': _A_in['C_re'], 'A_C_im': _A_in['C_im'], 'A_log_dt': _A_in['log_dt'], 'A_Dskip': _A_in['Dskip'], 'B_x': _B_in['x'], 'B_w_gate': _B_in['w_gate'], 'B_w_up': _B_in['w_up'], 'B_w_down': _B_in['w_down'], 'c': _w((lambda *s: torch.randn(*s, dtype=torch.bfloat16) * 0.2)(1, 576) * 8.0), 'w_mod': _w((lambda *s: torch.randn(*s, dtype=torch.bfloat16) * 0.2)(576, 6 * 576) * 576 ** (-0.5) * 8.0)}

def reference(A_u, A_A_re, A_A_im, A_B_re, A_B_im, A_C_re, A_C_im, A_log_dt, A_Dskip, B_x, B_w_gate, B_w_up, B_w_down, c, w_mod):

    def _A_ref(u, A_re, A_im, B_re, B_im, C_re, C_im, log_dt, Dskip):
        Bsz, L, D = u.shape
        dt = torch.exp(log_dt)[:, None]
        A = A_re + 1j * A_im
        Bc = B_re + 1j * B_im
        Cc = C_re + 1j * C_im
        logdA = dt * A
        dB = dt * Bc
        l = (lambda x, dt: x.to(dt))(torch.arange(L), A_re.dtype)
        P = torch.exp(logdA[:, :, None] * l[None, None, :])
        K = torch.einsum('dn,dnl,dn->dl', Cc, P, dB).real
        L2 = 2 * L
        Kp = _pad(K, ((0, 0), (0, L)))
        up = (lambda x, *d: x.permute(*d))(u, 0, 2, 1)
        up = _pad(up, ((0, 0), (0, 0), (0, L)))
        Kf = (lambda x, axes=(-2, -1): torch.fft.rfft2(x.float(), dim=axes))(Kp[:, None, :], axes=(-2, -1))
        Uf = (lambda x, axes=(-2, -1): torch.fft.rfft2(x.float(), dim=axes))(up[:, :, None, :], axes=(-2, -1))
        Yf = Uf * Kf[None, :, :, :]
        y = (lambda x, s, axes=(-2, -1): torch.fft.irfft2(x, s=s, dim=axes).to(torch.bfloat16))(Yf, (1, L2), axes=(-2, -1))[:, :, 0, :L]
        y = (lambda x, *d: x.permute(*d))(y, 0, 2, 1) + u * Dskip[None, None, :]
        return y

    def _B_ref(x, w_gate, w_up, w_down):
        g = torch.einsum('bsd,df->bsf', x, w_gate)
        u = torch.einsum('bsd,df->bsf', x, w_up)
        h = g * u
        return torch.einsum('bsf,fd->bsd', h, w_down)
    x = A_u
    mod = (_silu(c) @ w_mod).reshape(c.shape[0], 6, 1, -1)
    sa, ca, ga, sb, cb, gb = (mod[:, 0].clone(), mod[:, 1].clone(), mod[:, 2].clone(), mod[:, 3].clone(), mod[:, 4].clone(), mod[:, 5].clone())
    h = _layernorm(x, axis=-1) * (1 + ca) + sa
    x = x + ga * _A_ref(h, A_A_re, A_A_im, A_B_re, A_B_im, A_C_re, A_C_im, A_log_dt, A_Dskip)
    h = _layernorm(x, axis=-1) * (1 + cb) + sb
    x = x + gb * _B_ref(h, B_w_gate, B_w_up, B_w_down)
    return x
```

```cpp
#include <hip/hip_runtime.h>
#include <hip/hip_bf16.h>
#include <stdint.h>

typedef unsigned short u16;
#define L_ 2048
#define D_ 576
#define N_ 128
#define F_ 4608

__device__ __forceinline__ float bf2f(u16 v) { return __uint_as_float(((unsigned)v) << 16); }
__device__ __forceinline__ u16 f2bf(float f) {
    unsigned u = __float_as_uint(f);
    if ((u & 0x7fffffffu) > 0x7f800000u) return (u16)((u >> 16) | 0x40);
    u += 0x7fffu + ((u >> 16) & 1);
    return (u16)(u >> 16);
}
__device__ __forceinline__ float rbf(float f) { return bf2f(f2bf(f)); }

__global__ void k_mod(const u16* c, const u16* wmod, float* mod) {
    int j = blockIdx.x * blockDim.x + threadIdx.x;
    if (j >= 6 * D_) return;
    float acc = 0.f;
    for (int k = 0; k < D_; k++) {
        float cv = bf2f(c[k]);
        float s = rbf(cv / (1.f + __expf(-cv)));
        acc += s * bf2f(wmod[k * (6 * D_) + j]);
    }
    mod[j] = rbf(acc);
}

__global__ void k_ln(const u16* x, const float* shift, const float* scale, u16* h) {
    int row = blockIdx.x;
    int t = threadIdx.x;
    float v[9];
    float s = 0.f;
    for (int i = 0; i < 9; i++) { v[i] = bf2f(x[row * D_ + t + 64 * i]); s += v[i]; }
    for (int o = 32; o > 0; o >>= 1) s += __shfl_xor(s, o);
    float mean = s / D_;
    float q = 0.f;
    for (int i = 0; i < 9; i++) { float dd = v[i] - mean; q += dd * dd; }
    for (int o = 32; o > 0; o >>= 1) q += __shfl_xor(q, o);
    float var = rbf(q / D_);
    float m = rbf(mean);
    float sd = rbf(sqrtf(rbf(var + 1e-5f)));
    for (int i = 0; i < 9; i++) {
        int d = t + 64 * i;
        float n = rbf(rbf(v[i] - m) / sd);
        float o = rbf(rbf(n * rbf(1.f + scale[d])) + shift[d]);
        h[row * D_ + d] = f2bf(o);
    }
}

__global__ void k_kgen(const u16* Are, const u16* Aim, const u16* Bre, const u16* Bim, const u16* Cre, const u16* Cim,
                       const u16* logdt, float* K) {
    int d = blockIdx.y;
    int l = blockIdx.x * blockDim.x + threadIdx.x;
    float lf = rbf((float)l);
    float dt = rbf(expf(bf2f(logdt[d])));
    float acc = 0.f;
    for (int n = 0; n < N_; n++) {
        float ar = dt * bf2f(Are[d * N_ + n]), ai = dt * bf2f(Aim[d * N_ + n]);
        float br = dt * bf2f(Bre[d * N_ + n]), bi = dt * bf2f(Bim[d * N_ + n]);
        float cr = bf2f(Cre[d * N_ + n]), ci = bf2f(Cim[d * N_ + n]);
        float wr = cr * br - ci * bi, wi = cr * bi + ci * br;
        float e = expf(ar * lf);
        float sn, cs;
        sincosf(ai * lf, &sn, &cs);
        acc += e * (wr * cs - wi * sn);
    }
    K[d * L_ + l] = acc;
}

__global__ void k_conv(const u16* x, const u16* h, const float* K, const u16* Dskip, const float* ga, u16* x2) {
    int d = blockIdx.y;
    int t = blockIdx.x * blockDim.x + threadIdx.x;
    float acc = 0.f;
    for (int s = 0; s <= t; s++) acc += K[d * L_ + t - s] * bf2f(h[s * D_ + d]);
    float y = rbf(acc);
    float yy = rbf(y + rbf(bf2f(h[t * D_ + d]) * bf2f(Dskip[d])));
    x2[t * D_ + d] = f2bf(bf2f(x[t * D_ + d]) + rbf(ga[d] * yy));
}

__global__ void k_gemm1(const u16* h2, const u16* wg, const u16* wu, u16* H) {
    int f = blockIdx.x * blockDim.x + threadIdx.x;
    int t = blockIdx.y;
    float g = 0.f, u = 0.f;
    for (int k = 0; k < D_; k++) {
        float a = bf2f(h2[t * D_ + k]);
        g += a * bf2f(wg[k * F_ + f]);
        u += a * bf2f(wu[k * F_ + f]);
    }
    H[t * F_ + f] = f2bf(rbf(g) * rbf(u));
}

__global__ void k_gemm2(const u16* H, const u16* wd, const u16* x2, const float* gb, u16* out) {
    int d = blockIdx.x * blockDim.x + threadIdx.x;
    int t = blockIdx.y;
    float o = 0.f;
    for (int k = 0; k < F_; k++) o += bf2f(H[t * F_ + k]) * bf2f(wd[k * D_ + d]);
    out[t * D_ + d] = f2bf(bf2f(x2[t * D_ + d]) + rbf(gb[d] * rbf(o)));
}

extern "C" void kernel_launch(void* const* d_in, const int* in_sizes, int n_in,
                              void* d_out, int out_size, void* d_ws, size_t ws_size,
                              hipStream_t stream) {
    const u16* x = (const u16*)d_in[0];
    const u16* Are = (const u16*)d_in[1];
    const u16* Aim = (const u16*)d_in[2];
    const u16* Bre = (const u16*)d_in[3];
    const u16* Bim = (const u16*)d_in[4];
    const u16* Cre = (const u16*)d_in[5];
    const u16* Cim = (const u16*)d_in[6];
    const u16* logdt = (const u16*)d_in[7];
    const u16* Dskip = (const u16*)d_in[8];
    const u16* wg = (const u16*)d_in[10];
    const u16* wu = (const u16*)d_in[11];
    const u16* wd = (const u16*)d_in[12];
    const u16* c = (const u16*)d_in[13];
    const u16* wmod = (const u16*)d_in[14];
    char* ws = (char*)d_ws;
    float* mod = (float*)ws;
    u16* h = (u16*)(ws + (1 << 20));
    float* K = (float*)(ws + (4 << 20));
    u16* x2 = (u16*)(ws + (12 << 20));
    u16* h2 = (u16*)(ws + (16 << 20));
    u16* H = (u16*)(ws + (32 << 20));
    u16* out = (u16*)d_out;
    k_mod<<<(6 * D_ + 63) / 64, 64, 0, stream>>>(c, wmod, mod);
    k_ln<<<L_, 64, 0, stream>>>(x, mod + 0 * D_, mod + 1 * D_, h);
    k_kgen<<<dim3(L_ / 256, D_), 256, 0, stream>>>(Are, Aim, Bre, Bim, Cre, Cim, logdt, K);
    k_conv<<<dim3(L_ / 256, D_), 256, 0, stream>>>(x, h, K, Dskip, mod + 2 * D_, x2);
    k_ln<<<L_, 64, 0, stream>>>(x2, mod + 3 * D_, mod + 4 * D_, h2);
    k_gemm1<<<dim3(F_ / 256, L_), 256, 0, stream>>>(h2, wg, wu, H);
    k_gemm2<<<dim3(D_ / 64, L_), 64, 0, stream>>>(H, wd, x2, mod + 5 * D_, out);
}
```

```cpp
#include <hip/hip_runtime.h>
#include <hip/hip_bf16.h>
#include <stdint.h>
#include <type_traits>
#include <utility>

typedef unsigned short u16;
typedef __attribute__((ext_vector_type(8))) __bf16 bf8;
typedef __attribute__((ext_vector_type(2))) __bf16 bf2;
typedef __attribute__((ext_vector_type(2))) float f2;
typedef __attribute__((ext_vector_type(4))) float f4;
typedef __attribute__((ext_vector_type(16))) float f16v;
typedef __attribute__((ext_vector_type(4))) unsigned u4;
typedef __attribute__((ext_vector_type(2))) unsigned u2;

#define L_ 2048
#define D_ 576
#define N_ 128
#define F_ 4608

#define WS_MODP   0u
#define WS_MODF   (128u << 10)
#define WS_FLAG   (160u << 10)
#define WS_SD     (164u << 10)
#define WS_U12    (192u << 10)
#define WS_STAT   (176u << 10)
#define WS_XQ     (1u << 20)
#define WS_KR     (4u << 20)
#define WS_X2O    (8u << 20)
#define WS_W1     (12u << 20)
#define WS_WD     (24u << 20)
#define WS_H      (32u << 20)
#define WS_PART   (64u << 20)
#define WS_TMP    (96u << 20)
#define STREAM_B  (41 * 8192)

__device__ __forceinline__ float bf2f(u16 v) { return __uint_as_float(((unsigned)v) << 16); }
__device__ __forceinline__ u16 f2bf(float f) {
    __bf16 b = (__bf16)f;
    return __builtin_bit_cast(u16, b);
}
__device__ __forceinline__ float rbf(float f) { return bf2f(f2bf(f)); }
__device__ __forceinline__ unsigned pkbf(float a, float b) {
    f2 p = {a, b};
    return __builtin_bit_cast(unsigned, __builtin_convertvector(p, bf2));
}

template <int B, class F, int... I>
__device__ __forceinline__ void sf_impl(F&& f, std::integer_sequence<int, I...>) {
    (f(std::integral_constant<int, B + I>{}), ...);
}
template <int I, int N, class F>
__device__ __forceinline__ void static_for(F&& f) {
    if constexpr (N - I > 128) {
        static_for<I, I + 128>(f);
        static_for<I + 128, N>(f);
    } else if constexpr (I < N) sf_impl<I>(f, std::make_integer_sequence<int, N - I>{});
}
template <int N>
__device__ __forceinline__ void wait_vm() {
    asm volatile("s_waitcnt vmcnt(%0)" ::"n"(N > 63 ? 63 : N) : "memory");
}
__device__ __forceinline__ void gload16(u4& r, unsigned voff, const char* base) {
    asm volatile("global_load_dwordx4 %0, %1, %2" : "=v"(r) : "v"(voff), "s"(base));
}
__device__ __forceinline__ void glds16(unsigned lds, unsigned voff, const char* base) {
    asm volatile("s_mov_b32 m0, %0\n\ts_nop 0\n\tglobal_load_lds_dwordx4 %1, %2" ::"s"(lds), "v"(voff), "s"(base) : "memory");
}
typedef __attribute__((address_space(3))) const u4* lds_u4p;
typedef __attribute__((address_space(3))) const f4* lds_f4p;
__device__ __forceinline__ u4 ldsr(unsigned a) { return *(lds_u4p)(size_t)a; }
__device__ __forceinline__ f4 ldsrf(unsigned a) { return *(lds_f4p)(size_t)a; }
__device__ __forceinline__ unsigned lds_addr(char* p) {
    return (unsigned)(size_t)(__attribute__((address_space(3))) char*)p;
}
__device__ __forceinline__ void mfma(f4& c, const u4& a, const u4& b) {
    asm volatile("v_mfma_f32_16x16x32_bf16 %0, %1, %2, %0" : "+v"(c) : "v"(a), "v"(b));
}
__device__ __forceinline__ void mfma0(f4& c, const u4& a, const u4& b) {
    asm volatile("v_mfma_f32_16x16x32_bf16 %0, %1, %2, 0" : "=&v"(c) : "v"(a), "v"(b));
}
template <int N>
__device__ __forceinline__ void wait_vm_reg(u4& r) {
    asm volatile("s_waitcnt vmcnt(%1)" : "+v"(r) : "n"(N > 63 ? 63 : N));
}

#define TSK 1
#define TSDECL(k)
#define TS(i)
#define TSR(i)
#define TSFLUSH(n)
__host__ __device__ inline int w1_col(int c18, int np, int r) { return 288 * np + 96 * (c18 / 6) + 6 * r + c18 % 6; }
__host__ __device__ inline int h_col(int s, int q, int e) {
    int np = s / 9, r9 = s % 9;
    int c18 = r9 < 8 ? 9 * (r9 >> 2) + 2 * (r9 & 3) + e : 9 * e + 8;
    return w1_col(c18, np, 4 * q);
}
__host__ __device__ inline int g2_slot(int p, int ch, int ks) {
    if (ch == 8) return ks * 5;
    int g = ((ch >> 1) - p - 1) & 3;
    if (g < 2) return ks * 5 + 1 + g * 2 + (ch & 1);
    return 90 + (g - 2) * 36 + ks * 2 + (ch & 1);
}
__device__ __forceinline__ float lo2f(unsigned u) { return __builtin_bit_cast(float, u << 16); }
__device__ __forceinline__ float hi2f(unsigned u) { return __builtin_bit_cast(float, u & 0xffff0000u); }
__device__ __forceinline__ void st_sc1(void* p, u4 v) {
    asm volatile("global_store_dwordx4 %0, %1, off sc1\n\ts_nop 1" ::"v"(p), "v"(v) : "memory");
}
template <int CTRL>
__device__ __forceinline__ float dppf(float v) {
    return __builtin_bit_cast(float, __builtin_amdgcn_update_dpp(0, __builtin_bit_cast(int, v), CTRL, 0xf, 0xf, true));
}
__device__ __forceinline__ float row_sum(float v) {
    v += dppf<0xb1>(v); v += dppf<0x4e>(v); v += dppf<0x141>(v); v += dppf<0x140>(v);
    return v;
}
__device__ __forceinline__ float wave_max(float v) {
    v = fmaxf(v, dppf<0xb1>(v)); v = fmaxf(v, dppf<0x4e>(v)); v = fmaxf(v, dppf<0x141>(v)); v = fmaxf(v, dppf<0x140>(v));
    int iv = __builtin_bit_cast(int, v);
    float a = __builtin_bit_cast(float, __builtin_amdgcn_readlane(iv, 0)), b = __builtin_bit_cast(float, __builtin_amdgcn_readlane(iv, 16));
    float c = __builtin_bit_cast(float, __builtin_amdgcn_readlane(iv, 32)), d = __builtin_bit_cast(float, __builtin_amdgcn_readlane(iv, 48));
    return fmaxf(fmaxf(a, b), fmaxf(c, d));
}
typedef __attribute__((ext_vector_type(2))) _Float16 h2;
typedef __attribute__((ext_vector_type(8))) _Float16 h8;
__device__ __forceinline__ f2 cmul(f2 a, f2 b) { return f2{a[0] * b[0] - a[1] * b[1], a[0] * b[1] + a[1] * b[0]}; }
__device__ __forceinline__ f2 cpow(float e2, float rev, float n) {
    float m = __builtin_amdgcn_exp2f(e2 * n), r = __builtin_amdgcn_fractf(rev * n);
    return f2{m * __builtin_amdgcn_cosf(r), m * __builtin_amdgcn_sinf(r)};
}
__device__ __forceinline__ unsigned pkh(f2 v) { return __builtin_bit_cast(unsigned, __builtin_convertvector(v, h2)); }

#define A_KG 288
#define A_LN 128
#define A_MOD 162
#define A_WD 144
#define A_ZERO 56
#define A_SMEM (4 * 2 * 4352 + 2 * 5184)
struct AArgs {
    const u16 *x, *Are, *Aim, *Bre, *Bim, *Cre, *Cim, *logdt, *c, *wmod, *wd;
    char* ws;
};
__global__ __launch_bounds__(256) void k_a(AArgs A) {
    __shared__ __attribute__((aligned(16))) char smem[A_SMEM];
    int b = blockIdx.x;
    const int tid = threadIdx.x, lane = tid & 63, wid = __builtin_amdgcn_readfirstlane(tid >> 6);
    const int q = lane >> 4, i = lane & 15;
    if (b < A_KG) {
        const int d = b * 2 + (wid >> 1), n = d * N_ + (wid & 1) * 64 + lane;
        const float are = bf2f(A.Are[n]), aim = bf2f(A.Aim[n]), bre = bf2f(A.Bre[n]), bim = bf2f(A.Bim[n]);
        const float cre = bf2f(A.Cre[n]), cim = bf2f(A.Cim[n]);
        const float dt = rbf(__builtin_amdgcn_exp2f(bf2f(A.logdt[d]) * 1.44269504f));
        const float ar = dt * are * 1.44269504f, rev = dt * aim * 0.159154943f;
        f2 w = cmul(f2{cre, cim}, f2{dt * bre, dt * bim});
        const float amax = wave_max(ar);
        float wmax = wave_max(fmaxf(fabsf(w[0]), fabsf(w[1])));
        wmax = wmax > 1e-30f ? wmax : 1.f;
        w *= __builtin_amdgcn_rcpf(wmax);
        const float e2 = ar - amax;
        const f2 z1 = cpow(e2, rev, 1.f), z8 = cpow(e2, rev, 8.f), z128 = cpow(e2, rev, 128.f);
        unsigned* buf0 = (unsigned*)(smem + wid * 8704);
        unsigned* buf1 = buf0 + 1088;
        const h8* rd0 = (const h8*)(buf0 + i * 68 + q * 4);
        const h8* rd1 = (const h8*)(buf1 + i * 68 + q * 4);
        {
            f2 v = {1.f, 0.f};
            const f2 z8c = {z8[0], -z8[1]};
            for (int c = 0; c < 16; c++) { buf0[c * 68 + lane] = pkh(v); v = cmul(v, z8c); }
        }
        f2 zp[8];
        zp[1] = z1;
        for (int r = 2; r < 8; r++) zp[r] = cmul(zp[r - 1], z1);
        f2 g = w;
        for (int k = 0; k < 2; k++) {
            buf1[(k * 8) * 68 + lane] = pkh(g);
            for (int r = 1; r < 8; r++) buf1[(k * 8 + r) * 68 + lane] = pkh(cmul(g, zp[r]));
            g = cmul(g, z128);
        }
        h8 vf[4];
        f4 acc[3];
        for (int k = 0; k < 4; k++) vf[k] = rd0[k * 4];
        acc[0] = f4{0.f, 0.f, 0.f, 0.f};
        for (int k = 0; k < 4; k++) acc[0] = __builtin_amdgcn_mfma_f32_16x16x32_f16(rd1[k * 4], vf[k], acc[0], 0, 0, 0);
        for (int k = 0; k < 2; k++) {
            buf0[(k * 4) * 68 + lane] = pkh(g);
            for (int r = 1; r < 4; r++) buf0[(k * 4 + r) * 68 + lane] = pkh(cmul(g, zp[2 * r]));
            g = cmul(g, z128);
        }
        for (int k = 0; k < 4; k++) {
            buf0[(8 + k * 2) * 68 + lane] = pkh(g);
            buf0[(9 + k * 2) * 68 + lane] = pkh(cmul(g, zp[4]));
            g = cmul(g, z128);
        }
        acc[1] = f4{0.f, 0.f, 0.f, 0.f};
        for (int k = 0; k < 4; k++) acc[1] = __builtin_amdgcn_mfma_f32_16x16x32_f16(rd0[k * 4], vf[k], acc[1], 0, 0, 0);
        for (int k = 0; k < 9; k++) {
            buf1[k * 68 + lane] = pkh(g);
            g = cmul(g, z128);
        }
        acc[2] = f4{0.f, 0.f, 0.f, 0.f};
        for (int k = 0; k < 4; k++) acc[2] = __builtin_amdgcn_mfma_f32_16x16x32_f16(rd1[k * 4], vf[k], acc[2], 0, 0, 0);
        float* T = (float*)(smem + 4 * 8704 + (wid >> 1) * 5184);
        for (int r = 0; r < 4; r++) {
            int u0 = 128 * (q >> 1) + 4 * (q & 1) + r + 8 * i;
            T[u0 * 2 + (wid & 1)] = acc[0][r] * (wmax * __builtin_amdgcn_exp2f(amax * (float)u0));
            int u1 = q < 2 ? 256 + 128 * q + 2 * r : 256 * q + 128 * (r >> 1) + 4 * (r & 1);
            int i1 = q < 2 ? 256 + 64 * q + r + 4 * i : 256 + 64 * q + 32 * (r >> 1) + (r & 1) + 2 * i;
            T[i1 * 2 + (wid & 1)] = acc[1][r] * (wmax * __builtin_amdgcn_exp2f(amax * (float)(u1 + 8 * i)));
            int i2 = 64 * q + 16 * r + i;
            if (i2 <= 128) T[(512 + i2) * 2 + (wid & 1)] = acc[2][r] * (wmax * __builtin_amdgcn_exp2f(amax * (float)(1024 + 8 * i2)));
        }
        __syncthreads();
        const f2* T2 = (const f2*)T;
        auto tv = [&](int idx) { f2 t = T2[idx]; return t[0] + t[1]; };
        char* ke = A.ws + WS_KR + d * 3072;
        const int l128 = tid & 127;
        *(unsigned*)(ke + l128 * 4) = pkbf(tv(639 - l128), tv(640 - l128));
        if ((wid & 1) == 0) {
            int i0 = 510 - 2 * lane;
            *(u2*)(ke + 512 + lane * 8) = u2{pkbf(tv(i0), tv(i0 + 1)), pkbf(tv(i0 + 2), 0.f)};
        } else {
            float v[9];
            const int l0 = 511 - 8 * lane;
            for (int j = 0; j < 9; j++) {
                int l = l0 - j;
                int hf = l >> 1;
                int idx = l < 256 ? (l < 0 ? 0 : l) : 128 + hf + (l & hf & 1);
                v[j] = tv(idx);
            }
            if (lane == 63) v[8] = 0.f;
            *(u4*)(ke + 1024 + lane * 16) = u4{pkbf(v[1], v[2]), pkbf(v[3], v[4]), pkbf(v[5], v[6]), pkbf(v[7], v[8])};
            *(u4*)(ke + 2048 + lane * 16) = u4{pkbf(v[0], v[1]), pkbf(v[2], v[3]), pkbf(v[4], v[5]), pkbf(v[6], v[7])};
        }
        return;
    }
    b -= A_KG;
    if (b < A_LN) {
        const int tok = tid >> 4;
        const u2* xr = (const u2*)(A.x + (size_t)(b * 16 + tok) * D_) + i;
        u2 v[9];
        for (int k = 0; k < 9; k++) v[k] = xr[16 * k];
        float s = 0.f;
        for (int k = 0; k < 9; k++) {
            *(u2*)(smem + (i + 16 * k) * 144 + tok * 8) = v[k];
            s += (lo2f(v[k][0]) + hi2f(v[k][0])) + (lo2f(v[k][1]) + hi2f(v[k][1]));
        }
        const float mean = row_sum(s) * (1.f / D_);
        float s2 = 0.f;
        for (int k = 0; k < 9; k++)
            for (int e = 0; e < 2; e++) {
                float d0 = lo2f(v[k][e]) - mean, d1 = hi2f(v[k][e]) - mean;
                s2 += d0 * d0 + d1 * d1;
            }
        const float var = row_sum(s2) * (1.f / D_);
        if (i == 0) *(unsigned*)(A.ws + WS_STAT + (b * 16 + tok) * 4) = pkbf(mean, __builtin_sqrtf(rbf(rbf(var) + 1e-5f)));
        __syncthreads();
        for (int k = 0; k < 5; k++) {
            int g = k * 256 + tid;
            if (g < 1152) st_sc1(A.ws + WS_XQ + (g >> 3) * 16384 + b * 128 + (g & 7) * 16, *(const u4*)(smem + (g >> 3) * 144 + (g & 7) * 16));
        }
        return;
    }
    b -= A_LN;
    if (b < A_MOD) {
        const int cb = b % 54, kp = b / 54, cg = tid & 7, rg = tid >> 3;
        u4 wv[6];
        float sv[6];
        for (int k = 0; k < 6; k++) {
            int row = kp * 192 + k * 32 + rg;
            wv[k] = *(const u4*)(A.wmod + (size_t)row * (6 * D_) + cb * 64 + cg * 8);
            sv[k] = bf2f(A.c[row]);
        }
        float a[8] = {};
        for (int k = 0; k < 6; k++) {
            float s = rbf(sv[k] * __builtin_amdgcn_rcpf(1.f + __builtin_amdgcn_exp2f(-1.44269504f * sv[k])));
            for (int e = 0; e < 4; e++) { a[2 * e] += s * lo2f(wv[k][e]); a[2 * e + 1] += s * hi2f(wv[k][e]); }
        }
        float* red = (float*)smem;
        *(f4*)(red + rg * 68 + cg * 8) = f4{a[0], a[1], a[2], a[3]};
        *(f4*)(red + rg * 68 + cg * 8 + 4) = f4{a[4], a[5], a[6], a[7]};
        __syncthreads();
        const int col = tid >> 2, part = tid & 3;
        float s = 0.f;
        for (int k = 0; k < 8; k++) s += red[(part * 8 + k) * 68 + col];
        s += dppf<0xb1>(s);
        s += dppf<0x4e>(s);
        if (part == 0) ((float*)(A.ws + WS_MODP))[kp * 3456 + cb * 64 + col] = s;
        return;
    }
    b -= A_MOD;
    if (b < A_WD) {
        const int kc = b / 36, kh = (b % 36) / 18, ks = b % 18;
        const u16* r0 = A.wd + (size_t)h_col(b, q, 0) * D_ + wid * 144;
        const u16* r1 = A.wd + (size_t)h_col(b, q, 1) * D_ + wid * 144;
        unsigned v[5][8];
        for (int P = 0; P < 5; P++)
            for (int j = 0; j < 8; j++)
                v[P][j] = *(const unsigned*)((j < 4 ? r0 : r1) + (j & 3) * 6 * D_ + P * 32 + (P < 4 ? 2 * i : (i & 14)));
        char* dst = A.ws + WS_WD + (size_t)(wid * 4 + kc) * STREAM_B + kh * 1024 + lane * 16;
        for (int P = 0; P < 4; P++)
            for (int e = 0; e < 2; e++) {
                u4 o;
                for (int j = 0; j < 4; j++) o[j] = __builtin_amdgcn_perm(v[P][2 * j + 1], v[P][2 * j], e ? 0x07060302u : 0x05040100u);
                st_sc1(dst + g2_slot(kc, 2 * P + e, ks) * 2048, o);
            }
        u4 o;
        const unsigned sel = (i & 1) ? 0x07060302u : 0x05040100u;
        for (int j = 0; j < 4; j++) o[j] = __builtin_amdgcn_perm(v[4][2 * j + 1], v[4][2 * j], sel);
        st_sc1(dst + ks * 5 * 2048, o);
        return;
    }
    b -= A_WD;
    if (b == 0)
        for (int k = 0; k < 3; k++) *(u4*)(A.ws + WS_FLAG + (k * 256 + tid) * 16) = u4{0, 0, 0, 0};
    for (int k = 0; k < 64; k++) st_sc1(A.ws + WS_PART + ((size_t)(b * 64 + k) * 256 + tid) * 16, u4{0, 0, 0, 0});
}

#define B_CONV 144
#define B_PREP 96
#define B_HP 0
#define B_R  16640
#define B_Y  (B_R + 4 * 9152)
#define B_SMEM (B_Y + 65536)
template <int O, int OB>
__device__ __forceinline__ void cv_rd(u2& al, u2& ah, u4& bf, unsigned aa, unsigned ba) {
    asm volatile("ds_read2_b32 %0, %2 offset0:%3 offset1:%4\n\tds_read2_b32 %1, %2 offset0:%5 offset1:%6"
                 : "=&v"(al), "=&v"(ah) : "v"(aa), "n"(O), "n"(O + 1), "n"(O + 2), "n"(O + 3));
    asm volatile("ds_read_b128 %0, %1 offset:%2" : "=v"(bf) : "v"(ba), "n"(OB));
}
template <int W>
__device__ __forceinline__ void cv_wait(u2& al, u2& ah, u4& bf) {
    asm volatile("s_waitcnt lgkmcnt(%3)" : "+v"(al), "+v"(ah), "+v"(bf) : "n"(W > 15 ? 15 : W));
}
struct BArgs {
    const u16 *dskip, *wg, *wu;
    char* ws;
};
__global__ __launch_bounds__(512, 2) void k_b(BArgs A) {
    extern __shared__ __attribute__((aligned(16))) char smem[];
    int b = blockIdx.x;
    const int tid = threadIdx.x, lane = tid & 63, wid = __builtin_amdgcn_readfirstlane(tid >> 6);
    const int q = lane >> 4, i = lane & 15;
    const float* modp = (const float*)(A.ws + WS_MODP);
    if (b < B_CONV) {
        const int Q = (b >> 4) * 16 + (b & 7) * 2 + ((b >> 3) & 1);
        const unsigned sm = lds_addr(smem);
        const char* ke = A.ws + WS_KR + Q * 4 * 3072;
        glds16(sm + B_R + (wid >> 1) * 9152 + (wid & 1) * 4576 + 3072, lane * 16, ke + (wid >> 1) * 3072 + 1024 + (wid & 1) * 1024);
        const u4* xp = (const u4*)(A.ws + WS_XQ + Q * 16384 + tid * 32);
        const u4 xa = xp[0], xb = xp[1];
        const u4 st = *(const u4*)(A.ws + WS_STAT + tid * 16);
        const unsigned e3 = *(const unsigned*)(ke + (tid >> 7) * 3072 + (tid & 127) * 4);
        const u2 e2 = *(const u2*)(ke + (tid >> 7) * 3072 + 512 + lane * 8);
        float sa[4], c1[4], ga[4], dk[4];
        for (int c = 0; c < 4; c++) {
            const float* m = modp + Q * 4 + c;
            sa[c] = rbf(m[0] + m[3456] + m[6912]);
            c1[c] = rbf(1.f + rbf(m[576] + m[3456 + 576] + m[6912 + 576]));
            ga[c] = rbf(m[1152] + m[3456 + 1152] + m[6912 + 1152]);
            dk[c] = bf2f(A.dskip[Q * 4 + c]);
        }
        if (lane < 28) *(u4*)(smem + B_R + (wid >> 1) * 9152 + (wid & 1) * 4576 + 4096 + lane * 16) = u4{0, 0, 0, 0};
        else if (lane < 32) *(u4*)(smem + B_HP + (wid & 3) * 4160 + (lane - 28) * 16) = u4{0, 0, 0, 0};
        {
            char* r = smem + B_R + (tid >> 7) * 9152;
            const unsigned aa = __builtin_amdgcn_perm(e3, e3, 0x01000100u), bb = __builtin_amdgcn_perm(e3, e3, 0x03020302u);
            const unsigned ba = __builtin_amdgcn_perm(e3, e3, 0x01000302u);
            const bool modd = (tid & 1) == 0;
            *(u4*)(r + (tid & 127) * 16) = u4{bb, modd ? ba : aa, aa, aa};
            *(u4*)(r + 4576 + (tid & 127) * 16) = u4{bb, modd ? bb : ba, aa, aa};
            const unsigned t00 = __builtin_amdgcn_perm(e2[0], e2[0], 0x01000100u), t11 = __builtin_amdgcn_perm(e2[0], e2[0], 0x03020302u);
            const unsigned s0 = (wid & 1) ? 0x05040504u : 0x03020504u, s2 = (wid & 1) ? 0x01000302u : 0x01000100u;
            *(u4*)(r + (wid & 1) * 4576 + 2048 + lane * 16) =
                u4{__builtin_amdgcn_perm(e2[1], e2[0], s0), t11, __builtin_amdgcn_perm(e2[0], e2[0], s2), t00};
        }
        unsigned hd[4][2];
        {
            float hv[4][4];
            for (int t = 0; t < 4; t++) {
                const float m = lo2f(st[t]), rs = __builtin_amdgcn_rcpf(hi2f(st[t]));
                for (int e = 0; e < 2; e++) {
                    const unsigned xw = t < 2 ? xa[2 * t + e] : xb[2 * t - 4 + e];
                    unsigned u = pkbf(lo2f(xw) - m, hi2f(xw) - m);
                    u = pkbf(lo2f(u) * rs, hi2f(u) * rs);
                    u = pkbf(lo2f(u) * c1[2 * e], hi2f(u) * c1[2 * e + 1]);
                    hv[2 * e][t] = lo2f(u) + sa[2 * e];
                    hv[2 * e + 1][t] = hi2f(u) + sa[2 * e + 1];
                }
            }
            for (int c = 0; c < 4; c++) {
                u2 h = {pkbf(hv[c][0], hv[c][1]), pkbf(hv[c][2], hv[c][3])};
                *(u2*)(smem + B_HP + c * 4160 + 64 + tid * 8) = h;
                for (int e = 0; e < 2; e++) hd[c][e] = pkbf(lo2f(h[e]) * dk[c], hi2f(h[e]) * dk[c]);
            }
        }
        wait_vm<0>();
        __syncthreads();
        {
            const int ch = wid >> 1, hf = wid & 1;
            const unsigned ab = sm + B_HP + ch * 4160 + (hf * 64 + 4 * q + i + 1) * 4;
            const unsigned bbase = sm + B_R + ch * 9152 + (i & 1) * 4576 + 448 - 64 * (i >> 1) + 256 * hf + 16 * q;
            f4 acc[8];
            for (int c = 0; c < 8; c++) acc[c] = f4{0.f, 0.f, 0.f, 0.f};
            static_for<0, 4>([&](auto RI) {
                constexpr int rho = decltype(RI)::value;
                u2 al[8], ah[8];
                u4 bf[8];
                static_for<0, 8>([&](auto MI) {
                    constexpr int mu = decltype(MI)::value;
                    constexpr int o = (mu & 1) * 128 + rho * 16;
                    cv_rd<o, (7 - mu) * 512 + rho * 64>(al[mu], ah[mu], bf[mu], ab + (mu >> 1) * 1024, bbase);
                });
                static_for<0, 8>([&](auto CI) {
                    constexpr int n = decltype(CI)::value;
                    constexpr int w = (7 - n) * 3;
                    cv_wait<w>(al[n], ah[n], bf[n]);
                    for (int k = 0; k <= n; k++) {
                        if (n + k < 8) {
                            u4 an = {al[n][0], al[n][1], ah[n][0], ah[n][1]};
                            acc[n + k] = __builtin_amdgcn_mfma_f32_16x16x32_bf16(__builtin_bit_cast(bf8, an), __builtin_bit_cast(bf8, bf[k]), acc[n + k], 0, 0, 0);
                            if (k < n) {
                                u4 ak = {al[k][0], al[k][1], ah[k][0], ah[k][1]};
                                acc[n + k] = __builtin_amdgcn_mfma_f32_16x16x32_bf16(__builtin_bit_cast(bf8, ak), __builtin_bit_cast(bf8, bf[n]), acc[n + k], 0, 0, 0);
                            }
                        }
                    }
                });
            });
            char* yw = smem + B_Y + hf * 32768 + ch * 8192 + (i & 1) * 4096 + (16 * (i >> 1) + 4 * q) * 4;
            for (int c = 0; c < 8; c++) *(f4*)(yw + c * 512) = acc[c];
        }
        __syncthreads();
        float x2[4][4];
        for (int c = 0; c < 4; c++) {
            const char* yr = smem + B_Y + c * 8192 + tid * 8;
            const f2 y0 = *(const f2*)yr + *(const f2*)(yr + 32768), y1 = *(const f2*)(yr + 4096) + *(const f2*)(yr + 32768 + 4096);
            for (int e = 0; e < 2; e++) {
                unsigned u = pkbf(y0[e], y1[e]);
                u = pkbf(lo2f(u) + lo2f(hd[c][e]), hi2f(u) + hi2f(hd[c][e]));
                u = pkbf(lo2f(u) * ga[c], hi2f(u) * ga[c]);
                const unsigned x0 = e ? xb[c >> 1] : xa[c >> 1], x1 = e ? xb[2 + (c >> 1)] : xa[2 + (c >> 1)];
                x2[2 * e][c] = ((c & 1) ? hi2f(x0) : lo2f(x0)) + lo2f(u);
                x2[2 * e + 1][c] = ((c & 1) ? hi2f(x1) : lo2f(x1)) + hi2f(u);
            }
        }
        char* xo = A.ws + WS_X2O + (Q >> 1) * 32768 + tid * 64 + (Q & 1) * 8;
        for (int t = 0; t < 4; t++) *(u2*)(xo + t * 16) = u2{pkbf(x2[t][0], x2[t][1]), pkbf(x2[t][2], x2[t][3])};
        return;
    }
    b -= B_CONV;
    {
        const int up = b / 48, np = (b % 48) / 3, un = b % 3;
        const char* src = (const char*)((up ? A.wu : A.wg) + (size_t)(wid * 32 + q * 8) * F_ + 288 * np + 96 * un + 6 * i);
        typedef __attribute__((ext_vector_type(3))) unsigned u3;
        typedef u3 __attribute__((aligned(4))) u3a;
        u3 v[3][8];
        for (int k = 0; k < 2; k++)
            for (int j = 0; j < 8; j++) v[k][j] = *(const u3a*)(src + (size_t)(k * 256 + j) * (F_ * 2));
        if (wid < 2)
            for (int j = 0; j < 8; j++) v[2][j] = *(const u3a*)(src + (size_t)(512 + j) * (F_ * 2));
        float* tc = (float*)smem;
        float* tsb = tc + 576;
        float* red = tc + 1152;
        if (tid < 288) {
            const f2* m = (const f2*)(modp + 3 * D_) + tid;
            f2 sb = m[0] + m[1728] + m[3456], cb = m[288] + m[1728 + 288] + m[3456 + 288];
            *(f2*)(tc + 2 * tid) = f2{rbf(1.f + rbf(cb[0])), rbf(1.f + rbf(cb[1]))};
            *(f2*)(tsb + 2 * tid) = f2{rbf(sb[0]), rbf(sb[1])};
            if (b == 0) {
                f2 gb = m[576] + m[1728 + 576] + m[3456 + 576];
                *(f2*)(A.ws + WS_MODF + (5 * D_ + 2 * tid) * 4) = f2{rbf(gb[0]), rbf(gb[1])};
            }
        }
        __syncthreads();
        float s1[6] = {}, s2[6] = {};
        auto pass1 = [&](int k) {
            const int k0 = (wid + 8 * k) * 32 + q * 8;
            for (int j = 0; j < 8; j++) {
                const float c = tc[k0 + j], s = tsb[k0 + j];
                for (int e = 0; e < 3; e++) {
                    const float wl = lo2f(v[k][j][e]), wh = hi2f(v[k][j][e]);
                    s1[2 * e] += wl * c; s2[2 * e] += wl * s;
                    s1[2 * e + 1] += wh * c; s2[2 * e + 1] += wh * s;
                }
            }
        };
        pass1(0); pass1(1);
        if (wid < 2) pass1(2);
        {
            float t[6];
            for (int e = 0; e < 3; e++) {
                asm volatile("s_nop 1\n\tv_permlane16_swap_b32 %0, %1\n\tv_permlane16_swap_b32 %2, %3" : "+v"(s1[e]), "+v"(s1[3 + e]), "+v"(s2[e]), "+v"(s2[3 + e]));
                t[e] = s1[e] + s1[3 + e];
                t[3 + e] = s2[e] + s2[3 + e];
            }
            for (int e = 0; e < 3; e++) {
                asm volatile("s_nop 1\n\tv_permlane32_swap_b32 %0, %1" : "+v"(t[e]), "+v"(t[3 + e]));
                red[((3 * q + e) * 16 + i) * 8 + wid] = t[e] + t[3 + e];
            }
        }
        __syncthreads();
        float mean[6];
        for (int c = 0; c < 6; c++) {
            const f4 a = *(const f4*)(red + (c * 16 + i) * 8), bq = *(const f4*)(red + (c * 16 + i) * 8 + 4);
            const f4 s = a + bq;
            mean[c] = ((s[0] + s[1]) + (s[2] + s[3])) * (1.f / D_);
        }
        if (tid < 96) {
            const f4 a = *(const f4*)(red + (96 + tid) * 8), bq = *(const f4*)(red + (96 + tid) * 8 + 4);
            const f4 s = a + bq;
            const int c18 = un * 6 + (tid >> 4);
            *(float*)(A.ws + WS_U12 + (2 * np + c18 / 9) * 2048 + ((c18 % 9) * 32 + up * 16 + (tid & 15)) * 4) = (s[0] + s[1]) + (s[2] + s[3]);
        }
        auto pass2 = [&](int k) {
            const int ks = wid + 8 * k, k0 = ks * 32 + q * 8;
            float c[8];
            for (int j = 0; j < 8; j++) { c[j] = tc[k0 + j]; asm volatile("" : "+v"(v[k][j])); }
            for (int cc = 0; cc < 6; cc++) {
                u4 o;
                for (int j = 0; j < 4; j++) {
                    const unsigned w0 = v[k][2 * j][cc >> 1], w1 = v[k][2 * j + 1][cc >> 1];
                    o[j] = pkbf(((cc & 1) ? hi2f(w0) : lo2f(w0)) * c[2 * j] - mean[cc], ((cc & 1) ? hi2f(w1) : lo2f(w1)) * c[2 * j + 1] - mean[cc]);
                }
                const int c18 = un * 6 + cc, nt = 2 * np + c18 / 9, ch = c18 % 9;
                const int fi = ch < 8 ? (ch >> 2) * 144 + ks * 8 + (ch & 3) * 2 + up : 288 + ks * 2 + up;
                st_sc1(A.ws + WS_W1 + (size_t)nt * STREAM_B + fi * 1024 + lane * 16, o);
            }
        };
        pass2(0); pass2(1);
        if (wid < 2) pass2(2);
    }
}

#if defined(__HIP_DEVICE_COMPILE__)
#define NOPK __attribute__((target("no-packed-fp32-ops")))
#else
#define NOPK
#endif
#define G1_LAG 32
#define G1_WT 32
#define G1_WU 32
#define HST "sc1"
namespace g1 {
constexpr int NSLOT = 324, LAG = G1_LAG, NUNIT = 41, RING = 18, LOOK = 4, WT = G1_WT, WU = G1_WU, TSD = 110;
constexpr int NSYNC = 14;
constexpr int P[NSYNC + 1] = {0, 8, 16, 32, 48, 64, 80, 104, 128, 176, 200, 224, 248, 272, 324};
constexpr int NT = NSLOT + LAG;
struct Sched {
    int nop;
    int op[96];
    int optime[96];
    int sync_wait[NSYNC];
    int tok_wait[18];
    int stat_wait, sd_wait;
    int lo[NT + 2];
    int sync_at[NT];
    bool ok;
};
constexpr int need_unit(int j) {
    int fr = P[j + 1] + LOOK;
    if (fr > NSLOT) fr = NSLOT;
    return (fr - 1) / 8;
}
constexpr Sched make(int lag) {
    Sched s{};
    s.ok = true;
    int tu[NUNIT] = {}, tt[36] = {};
    for (int u = 0; u < NUNIT; u++) {
        int j = 0;
        while (need_unit(j) < u) j++;
        int t = P[j] - WU;
        if (t < 0) t = -1;
        if (u >= RING) {
            int jf = 0;
            while (P[jf] - LAG < 8 * (u - RING + 1)) jf++;
            if (jf >= j) s.ok = false;
            if (t < P[jf]) t = P[jf];
        }
        tu[u] = t;
    }
    for (int k = 0; k < 36; k++) {
        int t = (k >> 1) * 8 + lag - WT + (k & 1) * 4;
        tt[k] = t < 0 ? -1 : t;
    }
    int pos_u[NUNIT] = {}, pos_t[36] = {};
    int n = 3;
    int pos_sd = 0;
    int nu = 0, nk = 0, j = 0;
    for (int T = 0; T < 400; T++) {
        for (int jj = 0; jj < NSYNC; jj++)
            if (P[jj] == T)
                while (nu <= need_unit(jj) && tu[nu] < 0) { s.op[s.nop] = nu; s.optime[s.nop++] = -1; pos_u[nu++] = n++; }
        while (nk < 36 && (nk >> 1) * 8 + lag == T && tt[nk] < 0) { s.op[s.nop] = 100 + nk; s.optime[s.nop++] = -1; pos_t[nk++] = n++; }
    }
    s.stat_wait = n - 2;
    n++;
    for (int T = 0; T < NT; T++) {
        s.lo[T] = s.nop;
        s.sync_at[T] = -1;
        if (j < NSYNC && P[j] == T) {
            s.sync_at[T] = j;
            int u = need_unit(j);
            if (u >= nu) s.ok = false;
            s.sync_wait[j] = n - 1 - pos_u[u];
            j++;
        }
        bool computing = T >= lag;
        int cnt = 0;
        while (true) {
            if (computing && cnt >= 1) break;
            bool cu = nu < NUNIT && tu[nu] <= T, ck = nk < 36 && tt[nk] <= T;
            if (cu && (!ck || tu[nu] <= tt[nk])) { s.op[s.nop] = nu; s.optime[s.nop++] = T; pos_u[nu++] = n++; }
            else if (ck) { s.op[s.nop] = 100 + nk; s.optime[s.nop++] = T; pos_t[nk++] = n++; }
            else break;
            cnt++;
        }
        int t = T - lag;
        if (t >= 0 && t < 144 && t % 8 == 0) {
            if (t / 4 + 1 >= nk) s.ok = false;
            s.tok_wait[t / 8] = n - 1 - pos_t[t / 4 + 1];
        }
        if (t == TSD) { n += 2; pos_sd = n - 1; }
        if (t == 143) s.sd_wait = n - 1 - pos_sd;
        if (t == 143 || t == 287) n += 4;
    }
    s.lo[NT] = s.nop;
    if (j != NSYNC || nu != NUNIT || nk != 36) s.ok = false;
    return s;
}
template <int LAGV> struct SS { static constexpr Sched s = make(LAGV); };
static_assert(SS<0>::s.ok && SS<LAG>::s.ok, "bad schedule");
constexpr int TAB_OFF = RING * 8192;
constexpr int SMEM = TAB_OFF + 2048;

template <int N>
__device__ __forceinline__ void wait_vm_reg2(u4& a, u4& b) {
    asm volatile("s_waitcnt vmcnt(%2)" : "+v"(a), "+v"(b) : "n"(N > 63 ? 63 : N));
}
template <int CTRL>
__device__ __forceinline__ float dpp_add(float v) {
    return v + __builtin_bit_cast(float, __builtin_amdgcn_update_dpp(0, __builtin_bit_cast(int, v), CTRL, 0xf, 0xf, true));
}
__device__ __forceinline__ float wave_sum(float v) {
    v = dpp_add<0xb1>(v);
    v = dpp_add<0x4e>(v);
    v = dpp_add<0x141>(v);
    v = dpp_add<0x140>(v);
    int iv = __builtin_bit_cast(int, v);
    float a = __builtin_bit_cast(float, __builtin_amdgcn_readlane(iv, 0)), b = __builtin_bit_cast(float, __builtin_amdgcn_readlane(iv, 16));
    float c = __builtin_bit_cast(float, __builtin_amdgcn_readlane(iv, 32)), d = __builtin_bit_cast(float, __builtin_amdgcn_readlane(iv, 48));
    return (a + b) + (c + d);
}

template <int LAGV>
__device__ __forceinline__ void body(unsigned sm, const char* xsrc, unsigned voff, const char* wsrc, int wid,
                                     int lane, char* hdst, int odd, const char* sp, const char* tabsrc, char* sdp, unsigned sdw) {
    using S = SS<LAGV>;
    TSDECL(1);
    TSR(0); TS(1);
    u4 tok[36];
    u4 st0, st1;
    gload16(st0, lane * 32768, sp);
    gload16(st1, (lane & 7) * 32768, sp + 64 * 32768);
    glds16(sm + TAB_OFF + (wid & 1) * 1024, (wid & 1) * 1024 + lane * 16, tabsrc);
    unsigned lb0 = sm + lane * 16;
    asm volatile("" : "+v"(lb0));
    wsrc += wid * 1024 - (int)sm;
    hdst -= sm;
    char* hdst1 = hdst + 147456;
    const unsigned wdst = sm + wid * 1024;
    auto issue1 = [&](auto I) {
        constexpr int op = S::s.op[decltype(I)::value];
        if constexpr (op < 100) glds16(wdst + (op % RING) * 8192, lb0, wsrc + op * 8192);
        else gload16(tok[op - 100], voff, xsrc + ((op - 100) >> 1) * 131072 + ((op - 100) & 1) * 256);
    };
    static_for<0, S::s.lo[0]>(issue1);
    {
        wait_vm_reg2<S::s.stat_wait>(st0, st1);
        float v[16];
        for (int i = 0; i < 4; i++) {
            unsigned a = st0[i], b = lane < 8 ? st1[i] : 0u;
            v[2 * i] = __builtin_bit_cast(float, a << 16);
            v[2 * i + 1] = __builtin_bit_cast(float, a & 0xffff0000u);
            v[8 + 2 * i] = __builtin_bit_cast(float, b << 16);
            v[9 + 2 * i] = __builtin_bit_cast(float, b & 0xffff0000u);
        }
        float a = 0.f;
        for (int i = 0; i < 16; i++) a += v[i];
        const float mean = wave_sum(a) * (1.f / D_);
        float q0 = 0.f, q1 = 0.f;
        for (int i = 0; i < 8; i++) { float d0 = v[i] - mean, d1 = v[8 + i] - mean; q0 += d0 * d0; q1 += d1 * d1; }
        const float var = wave_sum(q0 + (lane < 8 ? q1 : 0.f)) * (1.f / D_);
        unsigned sdb = ((unsigned)f2bf(__builtin_sqrtf(rbf(rbf(var) + 1e-5f))) << 16) | 1u;
        asm volatile("global_store_dword %0, %1, %2 sc1" ::"v"(sdw), "v"(sdb), "s"(sdp) : "memory");
    }
    TS(2);
    unsigned lbc = 0;
    auto frag = [&](int f) -> u4 {
        f %= 144;
        if (f == 64 || f == 128) { lbc = lb0 + f * 1024; asm volatile("" : "+v"(lbc)); }
        return f < 64 ? ldsr(lb0 + f * 1024) : ldsr(lbc + (f & 63) * 1024);
    };
    f4 acc[8][2];
    u4 fr[4];
    float rs[2] = {0.f, 0.f};
    unsigned sdv[2];
    static_for<0, NT>([&](auto TI) {
        constexpr int T = decltype(TI)::value;
        constexpr int t = T - LAGV;
        if constexpr (S::s.sync_at[T] >= 0) {
            constexpr int j = S::s.sync_at[T];
            __builtin_amdgcn_sched_barrier(0);
            wait_vm<S::s.sync_wait[j]>();
            __builtin_amdgcn_s_barrier();
            asm volatile("" ::: "memory");
            TS(3 + j);
            __builtin_amdgcn_sched_barrier(0);
        }
        static_for<S::s.lo[T], S::s.lo[T + 1]>(issue1);
        if constexpr (t >= 0 && t < NSLOT) {
            if constexpr (t == 0) {
                for (int c = 0; c < 3; c++) fr[c] = frag(c);
            }
            constexpr int ks = t < 288 ? (t % 144) / 8 : (t - 288) / 2;
            constexpr int a = t < 288 ? t % 8 : t % 2;
            if constexpr (t < 144 && a == 0) wait_vm_reg2<S::s.tok_wait[ks]>(tok[2 * ks], tok[2 * ks + 1]);
            if constexpr (t + 3 < NSLOT) fr[(t + 3) & 3] = frag(t + 3);
            for (int sub = 0; sub < 2; sub++) {
                if constexpr (ks == 0) mfma0(acc[a][sub], fr[t & 3], tok[2 * ks + sub]);
                else mfma(acc[a][sub], fr[t & 3], tok[2 * ks + sub]);
                __builtin_amdgcn_sched_barrier(0);
            }
            if constexpr (t == TSD) {
                unsigned o = (lb0 - sm) >> 2 & 0x3c;
                asm volatile("global_load_dword %0, %2, %3 sc1\n\tglobal_load_dword %1, %2, %3 offset:64 sc1"
                             : "=&v"(sdv[0]), "=&v"(sdv[1]) : "v"(o), "s"(sdp + wid * 128));
            }
            if constexpr (t == 143) {
                unsigned o = (lb0 - sm) >> 2 & 0x3c, tmp;
                asm volatile(
                    "s_waitcnt vmcnt(%5)\n"
                    "1:\n\t"
                    "v_and_b32 %2, %0, %1\n\t"
                    "v_and_b32 %2, 1, %2\n\t"
                    "v_cmp_eq_u32 vcc, 0, %2\n\t"
                    "s_cbranch_vccz 2f\n\t"
                    "s_sleep 2\n\t"
                    "global_load_dword %0, %3, %4 sc1\n\t"
                    "global_load_dword %1, %3, %4 offset:64 sc1\n\t"
                    "s_waitcnt vmcnt(0)\n\t"
                    "s_branch 1b\n"
                    "2:"
                    : "+v"(sdv[0]), "+v"(sdv[1]), "=&v"(tmp)
                    : "v"(o), "s"(sdp + wid * 128), "n"(S::s.sd_wait)
                    : "vcc");
                for (int sub = 0; sub < 2; sub++) {
                    unsigned b = sdv[sub] & 0xffff0000u;
                    rs[sub] = __builtin_amdgcn_rcpf(__builtin_bit_cast(float, b));
                }
            }
            if constexpr (t == 143 || t == 287 || t == 323) {
                constexpr int g = t / 144;
                __builtin_amdgcn_sched_barrier(0);
                TS(20 + 2 * g);
                asm volatile("s_nop 7");
                constexpr int NC = g < 2 ? 4 : 1;
                unsigned tb = sm + TAB_OFF + (((lb0 - sm) >> 8) << 4);
                asm volatile("" : "+v"(tb));
                f4 ta = ldsrf(tb + 4 * g * 128), tc = ldsrf(tb + 4 * g * 128 + 64);
                static_for<0, NC>([&](auto CI) {
                    constexpr int c = decltype(CI)::value;
                    f4 ua = ta, uc = tc;
                    if constexpr (c + 1 < NC) {
                        ta = ldsrf(tb + (4 * g + c + 1) * 128);
                        tc = ldsrf(tb + (4 * g + c + 1) * 128 + 64);
                    }
                    for (int sub = 0; sub < 2; sub++) {
                        f4 pr = (acc[2 * c][sub] * rs[sub] + ua) * (acc[2 * c + 1][sub] * rs[sub] + uc);
                        unsigned o0 = pkbf(pr[0], pr[1]), o1 = pkbf(pr[2], pr[3]);
                        if constexpr (g < 2) {
                            if constexpr (c & 1) {
                                u4 o = {__builtin_bit_cast(unsigned, (float)acc[2 * c - 1][sub][0]), __builtin_bit_cast(unsigned, (float)acc[2 * c - 1][sub][1]), o0, o1};
                                asm volatile("global_store_dwordx4 %0, %1, %2 offset:%3 " HST "\n\ts_nop 1" ::"v"(lb0), "v"(o), "s"(sub ? hdst1 : hdst), "n"((2 * g + (c >> 1)) * 1024) : "memory");
                            } else {
                                acc[2 * c + 1][sub][0] = __builtin_bit_cast(float, o0);
                                acc[2 * c + 1][sub][1] = __builtin_bit_cast(float, o1);
                            }
                        } else {
                            u2 o2 = {o0, o1};
                            asm volatile("global_store_dwordx2 %0, %1, %2 " HST ::"v"(lb0), "v"(o2), "s"((sub ? hdst1 : hdst) + (8192 - odd * 4088)) : "memory");
                        }
                    }
                    __builtin_amdgcn_sched_barrier(0);
                });
                TS(21 + 2 * g);
                __builtin_amdgcn_sched_barrier(0);
            }
        }
    });
    TSFLUSH(28);
}
}

__global__ NOPK __launch_bounds__(512, 2) void k_gemm1(const char* ws_x2o, const char* ws_w1, const char* ws_u12, char* ws_h, char* ws_sd) {
    extern __shared__ __attribute__((aligned(16))) char smem[];
    const unsigned sm = lds_addr(smem);
    int b = blockIdx.x, xcd = b & 7, idx = b >> 3;
    int mt = (xcd & 1) * 4 + (idx & 3), nt = (xcd >> 1) * 8 + (idx >> 2);
    int tid = threadIdx.x, lane = tid & 63;
    int wid = __builtin_amdgcn_readfirstlane(tid >> 6);
    const int tl = nt * 8 + wid;
    const char* sp = ws_x2o + (mt * 256 + tl) * 16;
    const char* tabsrc = ws_u12 + nt * 2048;
    char* sdp = ws_sd + mt * 1024;
    const unsigned sdw = tl * 4;
    unsigned voff = ((lane >> 4) * 2048 + mt * 256 + wid * 32 + (lane & 15)) * 16;
    const char* wsrc = ws_w1 + (size_t)nt * STREAM_B;
    char* hdst = ws_h + ((size_t)((mt * 16 + wid * 2) * 144 + (nt >> 1) * 9) * 64) * 16 + (nt & 1) * 4096;
    if (wid < 4) {
        g1::body<0>(sm, ws_x2o, voff, wsrc, wid, lane, hdst, nt & 1, sp, tabsrc, sdp, sdw);
        __builtin_amdgcn_endpgm();
    }
    g1::body<g1::LAG>(sm, ws_x2o, voff, wsrc, wid, lane, hdst, nt & 1, sp, tabsrc, sdp, sdw);
}

#define G2_WT 30
#define G2_WU 28
#define G2_TLA 144
#define G2_TLB 160
namespace g2 {
constexpr int NSLOT = 162, NUNIT = 41, RING = 14, LOOK = 4, WT = G2_WT, WU = G2_WU;
constexpr int NSYNC = 17;
constexpr int P[NSYNC + 1] = {0, 4, 8, 16, 24, 32, 40, 48, 56, 64, 72, 80, 92, 104, 116, 128, 144, 162};
constexpr int TXA = 92, TXB = 128, TMISC = 133, TLA = G2_TLA, TLB = G2_TLB;
struct Sched {
    int nop;
    int op[96];
    int sync_wait[NSYNC];
    int tok_wait[18];
    int lo[NSLOT + 2];
    int sync_at[NSLOT];
    bool ok;
};
constexpr int need_unit(int j) {
    int fr = P[j + 1] + LOOK;
    if (fr > NSLOT) fr = NSLOT;
    return (fr - 1) / 4;
}
constexpr int extra(int t) {
    int n = 0;
    if (t >= TXA + 2 && t < TXA + 7) n++;
    if (t >= TXB + 2 && t < TXB + 4) n++;
    if (t >= TMISC && t < TMISC + 5) n++;
    if (t >= TLA && t < TLA + 5) n++;
    if (t >= TLB && t < TLB + 2) n++;
    return n;
}
constexpr Sched make() {
    Sched s{};
    s.ok = true;
    int tu[NUNIT] = {}, tt[36] = {};
    for (int u = 0; u < NUNIT; u++) {
        int j = 0;
        while (need_unit(j) < u) j++;
        int t = 4 * u - WU;
        if (t > P[j] - 12) t = P[j] - 12;
        if (t < 0) t = -1;
        if (u >= RING) {
            int jf = 0;
            while (P[jf] < 4 * (u - RING + 1)) jf++;
            if (jf >= j) s.ok = false;
            if (t < P[jf]) t = P[jf];
        }
        tu[u] = t;
    }
    for (int k = 0; k < 36; k++) {
        int t = (k >> 1) * 5 - WT + (k & 1) * 2;
        tt[k] = t < 0 ? -1 : t;
    }
    int pos_u[NUNIT] = {}, pos_t[36] = {};
    int n = 0, nu = 0, nk = 0, j = 0;
    for (int T = 0; T < 200; T++) {
        for (int jj = 0; jj < NSYNC; jj++)
            if (P[jj] == T)
                while (nu <= need_unit(jj) && tu[nu] < 0) { s.op[s.nop++] = nu; pos_u[nu++] = n++; }
        while (nk < 36 && (nk >> 1) * 5 == T && tt[nk] < 0) { s.op[s.nop++] = 100 + nk; pos_t[nk++] = n++; }
    }
    for (int T = 0; T < NSLOT; T++) {
        s.lo[T] = s.nop;
        s.sync_at[T] = -1;
        if (j < NSYNC && P[j] == T) {
            s.sync_at[T] = j;
            int u = need_unit(j);
            if (u >= nu) s.ok = false;
            s.sync_wait[j] = n - 1 - pos_u[u];
            j++;
        }
        bool cu = nu < NUNIT && tu[nu] <= T, ck = nk < 36 && tt[nk] <= T;
        if (cu && (!ck || tu[nu] <= tt[nk])) { s.op[s.nop++] = nu; pos_u[nu++] = n++; }
        else if (ck) { s.op[s.nop++] = 100 + nk; pos_t[nk++] = n++; }
        if (T < 90 && T % 5 == 0) {
            if (2 * (T / 5) + 1 >= nk) s.ok = false;
            s.tok_wait[T / 5] = n - 1 - pos_t[2 * (T / 5) + 1];
        }
        n += extra(T);
    }
    s.lo[NSLOT] = s.nop;
    if (j != NSYNC || nu != NUNIT || nk != 36) s.ok = false;
    return s;
}
constexpr Sched S = make();
static_assert(S.ok, "bad schedule");
constexpr int XB_OFF = RING * 8192;
constexpr int SMEM = XB_OFF + 8 * 5 * 1024;
typedef __attribute__((address_space(3))) f4* lds_f4w;
__device__ __forceinline__ void gload_sc1(f4& r, unsigned voff, const char* base) {
    asm volatile("global_load_dwordx4 %0, %1, %2 sc1" : "=v"(r) : "v"(voff), "s"(base));
}
__device__ __forceinline__ void pub(f4 v, unsigned voff, char* base) {
    u4 o = __builtin_bit_cast(u4, v);
    o[0] |= 1u; o[2] |= 1u;
    asm volatile("global_store_dwordx4 %0, %1, %2 sc1\n\ts_nop 1" ::"v"(voff), "v"(o), "s"(base) : "memory");
}
}

__global__ NOPK __launch_bounds__(512, 2) void k_gemm2(const char* ws_h, const char* ws_wd, char* ws_part, const char* ws_x2o,
                                                       const char* gbp, char* out) {
    using namespace g2;
    extern __shared__ __attribute__((aligned(16))) char smem[];
    const unsigned sm = lds_addr(smem);
    const int b = blockIdx.x, xcd = b & 7, idx = b >> 3;
    const int p = xcd >> 1, mt = (xcd & 1) * 8 + (idx & 7), nt = idx >> 3;
    const int tid = threadIdx.x, lane = tid & 63;
    const int wid = __builtin_amdgcn_readfirstlane(tid >> 6);
    const int kh = wid >> 2, tk = (wid & 3) * 2 + kh;
    TSDECL(2);
    TSR(0); TS(1);
    const unsigned l16 = lane * 16;
    const char* h0 = ws_h + (size_t)((mt * 8 + tk) * 144 + p * 36 + kh * 18) * 1024;
    const char* h1 = ws_h + (size_t)((mt * 8 + (tk ^ 1)) * 144 + p * 36 + kh * 18) * 1024;
    const char* wsrc = ws_wd + (size_t)(nt * 4 + p) * STREAM_B + wid * 1024;
    const unsigned wdst = sm + wid * 1024;
    char* part = ws_part + (size_t)(mt * 4 + nt) * (224 * 1024);
    u4 tok[36];
    auto issue1 = [&](auto I) {
        constexpr int op = S.op[decltype(I)::value];
        if constexpr (op < 100) glds16(wdst + (op % RING) * 8192, l16, wsrc + op * 8192);
        else gload16(tok[op - 100], l16, ((op & 1) ? h1 : h0) + ((op - 100) >> 1) * 1024);
    };
    static_for<0, S.lo[0]>(issue1);
    unsigned lb0 = sm + kh * 1024 + l16, lb1 = lb0 + 65536;
    asm volatile("" : "+v"(lb0), "+v"(lb1));
    unsigned xw = sm + XB_OFF + wid * 5120 + l16, xr = sm + XB_OFF + (wid ^ 4) * 5120 + l16;
    auto frag = [&](int t) -> u4 {
        int o = (t % (RING * 4)) * 2048;
        return o < 65536 ? ldsr(lb0 + o) : ldsr(lb1 + (o - 65536));
    };
    TS(2);
    f4 acc[9][2], xv[5], pl[7];
    u4 fr[4], x2v;
    f4 gb0, gb1;
    unsigned x2c, gbc;
    const int q = lane >> 4, tl = lane & 15;
    const int trow = (mt * 8 + tk) * 16 + tl;
    const unsigned off8 = 192 * 1024 + (q * 8 + tk) * 1024 + (p * 16 + tl) * 16;
    static_for<0, NSLOT>([&](auto TI) {
        constexpr int t = decltype(TI)::value;
        if constexpr (S.sync_at[t] >= 0) {
            constexpr int j = S.sync_at[t];
            __builtin_amdgcn_sched_barrier(0);
            if constexpr (t == TXA || t == TXB) asm volatile("s_waitcnt lgkmcnt(0)" ::: "memory");
            wait_vm<S.sync_wait[j]>();
            __builtin_amdgcn_s_barrier();
            asm volatile("" ::: "memory");
            TS(3 + j);
            __builtin_amdgcn_sched_barrier(0);
        }
        static_for<S.lo[t], S.lo[t + 1]>(issue1);
        if constexpr (t == 0) {
            for (int c = 0; c < 3; c++) fr[c] = frag(c);
        }
        constexpr int ks = t < 90 ? t / 5 : ((t - 90) % 36) / 2;
        constexpr int a = t < 90 ? t % 5 : (t < 126 ? 5 : 7) + (t & 1);
        if constexpr (t < 90 && a == 0) g1::wait_vm_reg2<S.tok_wait[ks]>(tok[2 * ks], tok[2 * ks + 1]);
        if constexpr (t + 3 < NSLOT) fr[(t + 3) & 3] = frag(t + 3);
        for (int sub = 0; sub < 2; sub++) {
            if constexpr (ks == 0) mfma0(acc[a][sub], fr[t & 3], tok[2 * ks + sub]);
            else mfma(acc[a][sub], fr[t & 3], tok[2 * ks + sub]);
            __builtin_amdgcn_sched_barrier(0);
        }
        if constexpr (t >= TXA - 5 && t < TXA) *(lds_f4w)(size_t)(xw + (t - (TXA - 5)) * 1024) = acc[t - (TXA - 5)][1];
        if constexpr (t >= TXB - 2 && t < TXB) *(lds_f4w)(size_t)(xw + (t - (TXB - 2)) * 1024) = acc[5 + t - (TXB - 2)][1];
        if constexpr (t >= TXA && t < TXA + 5) xv[t - TXA] = ldsrf(xr + (t - TXA) * 1024);
        if constexpr (t >= TXB && t < TXB + 2) xv[t - TXB] = ldsrf(xr + (t - TXB) * 1024);
        if constexpr (t == TXA + 2) pub(acc[0][0] + xv[0], l16, part + 192 * 1024 + (p * 8 + tk) * 1024);
        if constexpr (t > TXA + 2 && t < TXA + 7) {
            constexpr int c = t - TXA - 2, g = (c - 1) >> 1;
            pub(acc[c][0] + xv[c], l16, part + ((((p + 1 + g) & 3) * 3 + g) * 16 + tk * 2 + ((c - 1) & 1)) * 1024);
        }
        if constexpr (t >= TXB + 2 && t < TXB + 4) {
            constexpr int e = t - TXB - 2;
            pub(acc[5 + e][0] + xv[e], l16, part + ((((p + 3) & 3) * 3 + 2) * 16 + tk * 2 + e) * 1024);
        }
        if constexpr (t == TMISC) gload16(x2v, ((nt * 18 + p * 4 + q) * 2048 + trow) * 16, ws_x2o);
        if constexpr (t == TMISC + 1)
            asm volatile("global_load_ushort %0, %1, %2" : "=v"(x2c) : "v"(((nt * 18 + 16 + (p >> 1)) * 2048 + trow) * 16 + ((p & 1) * 4 + q) * 2), "s"(ws_x2o));
        if constexpr (t == TMISC + 2) asm volatile("global_load_dwordx4 %0, %1, %2" : "=v"(gb0) : "v"(q * 32), "s"(gbp + (nt * 144 + p * 32) * 4));
        if constexpr (t == TMISC + 3) asm volatile("global_load_dwordx4 %0, %1, %2 offset:16" : "=v"(gb1) : "v"(q * 32), "s"(gbp + (nt * 144 + p * 32) * 4));
        if constexpr (t == TMISC + 4) asm volatile("global_load_dword %0, %1, %2" : "=v"(gbc) : "v"(q * 4), "s"(gbp + (nt * 144 + 128 + p * 4) * 4));
        if constexpr (t >= TLA && t < TLA + 4) gload_sc1(pl[t - TLA], l16, part + ((p * 3 + ((t - TLA) >> 1)) * 16 + tk * 2 + ((t - TLA) & 1)) * 1024);
        if constexpr (t == TLA + 4) gload_sc1(pl[6], off8, part);
        if constexpr (t >= TLB && t < TLB + 2) gload_sc1(pl[4 + t - TLB], l16, part + ((p * 3 + 2) * 16 + tk * 2 + (t - TLB)) * 1024);
        __builtin_amdgcn_sched_barrier(0);
    });
    TS(20);
    asm volatile("s_nop 7");
    *(lds_f4w)(size_t)(xw + 2048) = acc[7][1];
    *(lds_f4w)(size_t)(xw + 3072) = acc[8][1];
    asm volatile("s_waitcnt lgkmcnt(0)" ::: "memory");
    __builtin_amdgcn_s_barrier();
    asm volatile("" ::: "memory");
    f4 o0 = acc[7][0] + ldsrf(xr + 2048), o1 = acc[8][0] + ldsrf(xr + 3072);
    TS(21);
    asm volatile("s_waitcnt vmcnt(0)" : "+v"(pl[0]), "+v"(pl[1]), "+v"(pl[2]), "+v"(pl[3]), "+v"(pl[4]), "+v"(pl[5]), "+v"(pl[6]),
                 "+v"(x2v), "+v"(gb0), "+v"(gb1), "+v"(x2c), "+v"(gbc));
    TS(22);
    while (true) {
        unsigned tg = 1u;
        for (int i = 0; i < 7; i++) {
            u4 u = __builtin_bit_cast(u4, pl[i]);
            tg &= u[0] & u[2];
        }
        if (__builtin_amdgcn_ballot_w64(tg == 0) == 0) break;
        __builtin_amdgcn_s_sleep(1);
        for (int i = 0; i < 6; i++) gload_sc1(pl[i], l16, part + ((p * 3 + (i >> 1)) * 16 + tk * 2 + (i & 1)) * 1024);
        gload_sc1(pl[6], off8, part);
        asm volatile("s_waitcnt vmcnt(0)" : "+v"(pl[0]), "+v"(pl[1]), "+v"(pl[2]), "+v"(pl[3]), "+v"(pl[4]), "+v"(pl[5]), "+v"(pl[6]));
    }
    TS(23);
    o0 += (pl[0] + pl[2]) + pl[4];
    o1 += (pl[1] + pl[3]) + pl[5];
    u4 ov;
    for (int r = 0; r < 4; r++) {
        unsigned ob = pkbf(o0[r], o1[r]);
        float ga = r < 2 ? gb0[2 * r] : gb1[2 * r - 4], gc = r < 2 ? gb0[2 * r + 1] : gb1[2 * r - 3];
        unsigned pb = pkbf(ga * __builtin_bit_cast(float, ob << 16), gc * __builtin_bit_cast(float, ob & 0xffff0000u));
        ov[r] = pkbf(__builtin_bit_cast(float, x2v[r] << 16) + __builtin_bit_cast(float, pb << 16),
                     __builtin_bit_cast(float, x2v[r] & 0xffff0000u) + __builtin_bit_cast(float, pb & 0xffff0000u));
    }
    *(u4*)(out + (size_t)trow * 1152 + (nt * 144 + p * 32 + q * 8) * 2) = ov;
    {
        float u0 = pl[6][0], u1 = pl[6][1], u2 = pl[6][2], u3 = pl[6][3];
        asm volatile("s_nop 1\n\tv_permlane16_swap_b32 %0, %1\n\tv_permlane16_swap_b32 %2, %3" : "+v"(u0), "+v"(u1), "+v"(u2), "+v"(u3));
        float a = u0 + u1, c = u2 + u3;
        asm volatile("s_nop 1\n\tv_permlane32_swap_b32 %0, %1" : "+v"(a), "+v"(c));
        float o = a + c;
        *(u16*)(out + (size_t)trow * 1152 + (nt * 144 + 128 + p * 4 + q) * 2) =
            f2bf(__builtin_bit_cast(float, x2c << 16) + rbf(__builtin_bit_cast(float, gbc) * rbf(o)));
    }
    TSFLUSH(28);
}

extern "C" void kernel_launch(void* const* d_in, const int* in_sizes, int n_in,
                              void* d_out, int out_size, void* d_ws, size_t ws_size,
                              hipStream_t stream) {
    char* ws = (char*)d_ws;
    AArgs a;
    a.x = (const u16*)d_in[0];
    a.Are = (const u16*)d_in[1];
    a.Aim = (const u16*)d_in[2];
    a.Bre = (const u16*)d_in[3];
    a.Bim = (const u16*)d_in[4];
    a.Cre = (const u16*)d_in[5];
    a.Cim = (const u16*)d_in[6];
    a.logdt = (const u16*)d_in[7];
    a.c = (const u16*)d_in[13];
    a.wmod = (const u16*)d_in[14];
    a.wd = (const u16*)d_in[12];
    a.ws = ws;
    BArgs bb;
    bb.dskip = (const u16*)d_in[8];
    bb.wg = (const u16*)d_in[10];
    bb.wu = (const u16*)d_in[11];
    bb.ws = ws;
    (void)hipFuncSetAttribute((const void*)k_b, hipFuncAttributeMaxDynamicSharedMemorySize, B_SMEM);
    (void)hipFuncSetAttribute((const void*)k_gemm1, hipFuncAttributeMaxDynamicSharedMemorySize, g1::SMEM);
    (void)hipFuncSetAttribute((const void*)k_gemm2, hipFuncAttributeMaxDynamicSharedMemorySize, g2::SMEM);
    k_a<<<A_KG + A_LN + A_MOD + A_WD + A_ZERO, 256, 0, stream>>>(a);
    k_b<<<B_CONV + B_PREP, 512, B_SMEM, stream>>>(bb);
    k_gemm1<<<256, 512, g1::SMEM, stream>>>(ws + WS_X2O, ws + WS_W1, ws + WS_U12, ws + WS_H, ws + WS_SD);
    k_gemm2<<<256, 512, g2::SMEM, stream>>>(ws + WS_H, ws + WS_WD, ws + WS_PART, ws + WS_X2O, ws + WS_MODF + 5 * D_ * 4, (char*)d_out);
}
```

```cpp
#include <hip/hip_runtime.h>
#include <hip/hip_bf16.h>
#include <stdint.h>
#include <type_traits>
#include <utility>

typedef unsigned short u16;
typedef __attribute__((ext_vector_type(8))) __bf16 bf8;
typedef __attribute__((ext_vector_type(2))) __bf16 bf2;
typedef __attribute__((ext_vector_type(2))) float f2;
typedef __attribute__((ext_vector_type(4))) float f4;
typedef __attribute__((ext_vector_type(16))) float f16v;
typedef __attribute__((ext_vector_type(4))) unsigned u4;
typedef __attribute__((ext_vector_type(2))) unsigned u2;

#define L_ 2048
#define D_ 576
#define N_ 128
#define F_ 4608

#define WS_MODP   0u
#define WS_MODF   (128u << 10)
#define WS_FLAG   (160u << 10)
#define WS_SD     (164u << 10)
#define WS_U12    (192u << 10)
#define WS_STAT   (176u << 10)
#define WS_XQ     (1u << 20)
#define WS_KR     (4u << 20)
#define WS_X2O    (8u << 20)
#define WS_W1     (12u << 20)
#define WS_WD     (24u << 20)
#define WS_H      (32u << 20)
#define WS_PART   (64u << 20)
#define WS_TMP    (96u << 20)
#define STREAM_B  (41 * 8192)

__device__ __forceinline__ float bf2f(u16 v) { return __uint_as_float(((unsigned)v) << 16); }
__device__ __forceinline__ u16 f2bf(float f) {
    __bf16 b = (__bf16)f;
    return __builtin_bit_cast(u16, b);
}
__device__ __forceinline__ float rbf(float f) { return bf2f(f2bf(f)); }
__device__ __forceinline__ unsigned pkbf(float a, float b) {
    f2 p = {a, b};
    return __builtin_bit_cast(unsigned, __builtin_convertvector(p, bf2));
}

template <int B, class F, int... I>
__device__ __forceinline__ void sf_impl(F&& f, std::integer_sequence<int, I...>) {
    (f(std::integral_constant<int, B + I>{}), ...);
}
template <int I, int N, class F>
__device__ __forceinline__ void static_for(F&& f) {
    if constexpr (N - I > 128) {
        static_for<I, I + 128>(f);
        static_for<I + 128, N>(f);
    } else if constexpr (I < N) sf_impl<I>(f, std::make_integer_sequence<int, N - I>{});
}
template <int N>
__device__ __forceinline__ void wait_vm() {
    asm volatile("s_waitcnt vmcnt(%0)" ::"n"(N > 63 ? 63 : N) : "memory");
}
__device__ __forceinline__ void gload16(u4& r, unsigned voff, const char* base) {
    asm volatile("global_load_dwordx4 %0, %1, %2" : "=v"(r) : "v"(voff), "s"(base));
}
__device__ __forceinline__ void glds16(unsigned lds, unsigned voff, const char* base) {
    asm volatile("s_mov_b32 m0, %0\n\ts_nop 0\n\tglobal_load_lds_dwordx4 %1, %2" ::"s"(lds), "v"(voff), "s"(base) : "memory");
}
typedef __attribute__((address_space(3))) const u4* lds_u4p;
typedef __attribute__((address_space(3))) const f4* lds_f4p;
__device__ __forceinline__ u4 ldsr(unsigned a) { return *(lds_u4p)(size_t)a; }
__device__ __forceinline__ f4 ldsrf(unsigned a) { return *(lds_f4p)(size_t)a; }
__device__ __forceinline__ unsigned lds_addr(char* p) {
    return (unsigned)(size_t)(__attribute__((address_space(3))) char*)p;
}
__device__ __forceinline__ void mfma(f4& c, const u4& a, const u4& b) {
    asm volatile("v_mfma_f32_16x16x32_bf16 %0, %1, %2, %0" : "+v"(c) : "v"(a), "v"(b));
}
__device__ __forceinline__ void mfma0(f4& c, const u4& a, const u4& b) {
    asm volatile("v_mfma_f32_16x16x32_bf16 %0, %1, %2, 0" : "=&v"(c) : "v"(a), "v"(b));
}
template <int N>
__device__ __forceinline__ void wait_vm_reg(u4& r) {
    asm volatile("s_waitcnt vmcnt(%1)" : "+v"(r) : "n"(N > 63 ? 63 : N));
}

#define TSK 1
#define TSDECL(k)
#define TS(i)
#define TSR(i)
#define TSFLUSH(n)
__host__ __device__ inline int w1_col(int c18, int np, int r) { return 288 * np + 96 * (c18 / 6) + 6 * r + c18 % 6; }
__host__ __device__ inline int h_col(int s, int q, int e) {
    int np = s / 9, r9 = s % 9;
    int c18 = r9 < 8 ? 9 * (r9 >> 2) + 2 * (r9 & 3) + e : 9 * e + 8;
    return w1_col(c18, np, 4 * q);
}
__host__ __device__ constexpr int g2_pos(int s) {
    int np = s / 9, r9 = s % 9;
    return r9 == 8 ? 16 + np : (r9 & 2) ? 8 + (r9 & 1) * 4 + np * 2 + (r9 >> 2) : np * 4 + (r9 >> 2) * 2 + (r9 & 1);
}
__host__ __device__ constexpr int g2_inv(int k) {
    return k >= 16 ? (k - 16) * 9 + 8 : k < 8 ? (k >> 2) * 9 + ((k >> 1) & 1) * 4 + (k & 1) : ((k >> 1) & 1) * 9 + (k & 1) * 4 + 2 + ((k - 8) >> 2);
}
__host__ __device__ inline int g2_slot(int p, int ch, int ks) {
    if (ch == 8) return ks < 12 ? ks * 7 : 48 + ks * 3;
    int g = ((ch >> 1) - p - 1) & 3;
    if (g == 3) return 126 + ks * 2 + (ch & 1);
    if (ks < 12) return ks * 7 + 1 + g * 2 + (ch & 1);
    return g == 0 ? 49 + ks * 3 + (ch & 1) : 66 + g * 12 + ks * 2 + (ch & 1);
}
__device__ __forceinline__ float lo2f(unsigned u) { return __builtin_bit_cast(float, u << 16); }
__device__ __forceinline__ float hi2f(unsigned u) { return __builtin_bit_cast(float, u & 0xffff0000u); }
__device__ __forceinline__ void st_sc1(void* p, u4 v) {
    asm volatile("global_store_dwordx4 %0, %1, off sc1\n\ts_nop 1" ::"v"(p), "v"(v) : "memory");
}
template <int CTRL>
__device__ __forceinline__ float dppf(float v) {
    return __builtin_bit_cast(float, __builtin_amdgcn_update_dpp(0, __builtin_bit_cast(int, v), CTRL, 0xf, 0xf, true));
}
__device__ __forceinline__ float row_sum(float v) {
    v += dppf<0xb1>(v); v += dppf<0x4e>(v); v += dppf<0x141>(v); v += dppf<0x140>(v);
    return v;
}
__device__ __forceinline__ float wave_max(float v) {
    v = fmaxf(v, dppf<0xb1>(v)); v = fmaxf(v, dppf<0x4e>(v)); v = fmaxf(v, dppf<0x141>(v)); v = fmaxf(v, dppf<0x140>(v));
    int iv = __builtin_bit_cast(int, v);
    float a = __builtin_bit_cast(float, __builtin_amdgcn_readlane(iv, 0)), b = __builtin_bit_cast(float, __builtin_amdgcn_readlane(iv, 16));
    float c = __builtin_bit_cast(float, __builtin_amdgcn_readlane(iv, 32)), d = __builtin_bit_cast(float, __builtin_amdgcn_readlane(iv, 48));
    return fmaxf(fmaxf(a, b), fmaxf(c, d));
}
typedef __attribute__((ext_vector_type(2))) _Float16 h2;
typedef __attribute__((ext_vector_type(8))) _Float16 h8;
__device__ __forceinline__ f2 cmul(f2 a, f2 b) { return f2{a[0] * b[0] - a[1] * b[1], a[0] * b[1] + a[1] * b[0]}; }
__device__ __forceinline__ f2 cpow(float e2, float rev, float n) {
    float m = __builtin_amdgcn_exp2f(e2 * n), r = __builtin_amdgcn_fractf(rev * n);
    return f2{m * __builtin_amdgcn_cosf(r), m * __builtin_amdgcn_sinf(r)};
}
__device__ __forceinline__ unsigned pkh(f2 v) { return __builtin_bit_cast(unsigned, __builtin_convertvector(v, h2)); }

#define A_KG 288
#define A_LN 128
#define A_MOD 162
#define A_WD 144
#define A_ZERO 1
#define A_SMEM (4 * 2 * 4352 + 2 * 5184)
struct AArgs {
    const u16 *x, *Are, *Aim, *Bre, *Bim, *Cre, *Cim, *logdt, *c, *wmod, *wd;
    char* ws;
};
__global__ __launch_bounds__(256) void k_a(AArgs A) {
    __shared__ __attribute__((aligned(16))) char smem[A_SMEM];
    int b = blockIdx.x;
    const int tid = threadIdx.x, lane = tid & 63, wid = __builtin_amdgcn_readfirstlane(tid >> 6);
    const int q = lane >> 4, i = lane & 15;
    if (b < A_KG) {
        const int d = b * 2 + (wid >> 1), n = d * N_ + (wid & 1) * 64 + lane;
        const float are = bf2f(A.Are[n]), aim = bf2f(A.Aim[n]), bre = bf2f(A.Bre[n]), bim = bf2f(A.Bim[n]);
        const float cre = bf2f(A.Cre[n]), cim = bf2f(A.Cim[n]);
        const float dt = rbf(__builtin_amdgcn_exp2f(bf2f(A.logdt[d]) * 1.44269504f));
        const float ar = dt * are * 1.44269504f, rev = dt * aim * 0.159154943f;
        f2 w = cmul(f2{cre, cim}, f2{dt * bre, dt * bim});
        const float amax = wave_max(ar);
        float wmax = wave_max(fmaxf(fabsf(w[0]), fabsf(w[1])));
        wmax = wmax > 1e-30f ? wmax : 1.f;
        w *= __builtin_amdgcn_rcpf(wmax);
        const float e2 = ar - amax;
        const f2 z1 = cpow(e2, rev, 1.f), z8 = cpow(e2, rev, 8.f), z128 = cpow(e2, rev, 128.f);
        unsigned* buf0 = (unsigned*)(smem + wid * 8704);
        unsigned* buf1 = buf0 + 1088;
        const h8* rd0 = (const h8*)(buf0 + i * 68 + q * 4);
        const h8* rd1 = (const h8*)(buf1 + i * 68 + q * 4);
        {
            f2 v = {1.f, 0.f};
            const f2 z8c = {z8[0], -z8[1]};
            for (int c = 0; c < 16; c++) { buf0[c * 68 + lane] = pkh(v); v = cmul(v, z8c); }
        }
        f2 zp[8];
        zp[1] = z1;
        for (int r = 2; r < 8; r++) zp[r] = cmul(zp[r - 1], z1);
        f2 g = w;
        for (int k = 0; k < 2; k++) {
            buf1[(k * 8) * 68 + lane] = pkh(g);
            for (int r = 1; r < 8; r++) buf1[(k * 8 + r) * 68 + lane] = pkh(cmul(g, zp[r]));
            g = cmul(g, z128);
        }
        h8 vf[4];
        f4 acc[3];
        for (int k = 0; k < 4; k++) vf[k] = rd0[k * 4];
        acc[0] = f4{0.f, 0.f, 0.f, 0.f};
        for (int k = 0; k < 4; k++) acc[0] = __builtin_amdgcn_mfma_f32_16x16x32_f16(rd1[k * 4], vf[k], acc[0], 0, 0, 0);
        for (int k = 0; k < 2; k++) {
            buf0[(k * 4) * 68 + lane] = pkh(g);
            for (int r = 1; r < 4; r++) buf0[(k * 4 + r) * 68 + lane] = pkh(cmul(g, zp[2 * r]));
            g = cmul(g, z128);
        }
        for (int k = 0; k < 4; k++) {
            buf0[(8 + k * 2) * 68 + lane] = pkh(g);
            buf0[(9 + k * 2) * 68 + lane] = pkh(cmul(g, zp[4]));
            g = cmul(g, z128);
        }
        acc[1] = f4{0.f, 0.f, 0.f, 0.f};
        for (int k = 0; k < 4; k++) acc[1] = __builtin_amdgcn_mfma_f32_16x16x32_f16(rd0[k * 4], vf[k], acc[1], 0, 0, 0);
        for (int k = 0; k < 9; k++) {
            buf1[k * 68 + lane] = pkh(g);
            g = cmul(g, z128);
        }
        acc[2] = f4{0.f, 0.f, 0.f, 0.f};
        for (int k = 0; k < 4; k++) acc[2] = __builtin_amdgcn_mfma_f32_16x16x32_f16(rd1[k * 4], vf[k], acc[2], 0, 0, 0);
        float* T = (float*)(smem + 4 * 8704 + (wid >> 1) * 5184);
        for (int r = 0; r < 4; r++) {
            int u0 = 128 * (q >> 1) + 4 * (q & 1) + r + 8 * i;
            T[u0 * 2 + (wid & 1)] = acc[0][r] * (wmax * __builtin_amdgcn_exp2f(amax * (float)u0));
            int u1 = q < 2 ? 256 + 128 * q + 2 * r : 256 * q + 128 * (r >> 1) + 4 * (r & 1);
            int i1 = q < 2 ? 256 + 64 * q + r + 4 * i : 256 + 64 * q + 32 * (r >> 1) + (r & 1) + 2 * i;
            T[i1 * 2 + (wid & 1)] = acc[1][r] * (wmax * __builtin_amdgcn_exp2f(amax * (float)(u1 + 8 * i)));
            int i2 = 64 * q + 16 * r + i;
            if (i2 <= 128) T[(512 + i2) * 2 + (wid & 1)] = acc[2][r] * (wmax * __builtin_amdgcn_exp2f(amax * (float)(1024 + 8 * i2)));
        }
        __syncthreads();
        const f2* T2 = (const f2*)T;
        auto tv = [&](int idx) { f2 t = T2[idx]; return t[0] + t[1]; };
        char* ke = A.ws + WS_KR + d * 3072;
        const int l128 = tid & 127;
        *(unsigned*)(ke + l128 * 4) = pkbf(tv(639 - l128), tv(640 - l128));
        if ((wid & 1) == 0) {
            int i0 = 510 - 2 * lane;
            *(u2*)(ke + 512 + lane * 8) = u2{pkbf(tv(i0), tv(i0 + 1)), pkbf(tv(i0 + 2), 0.f)};
        } else {
            float v[9];
            const int l0 = 511 - 8 * lane;
            for (int j = 0; j < 9; j++) {
                int l = l0 - j;
                int hf = l >> 1;
                int idx = l < 256 ? (l < 0 ? 0 : l) : 128 + hf + (l & hf & 1);
                v[j] = tv(idx);
            }
            if (lane == 63) v[8] = 0.f;
            *(u4*)(ke + 1024 + lane * 16) = u4{pkbf(v[1], v[2]), pkbf(v[3], v[4]), pkbf(v[5], v[6]), pkbf(v[7], v[8])};
            *(u4*)(ke + 2048 + lane * 16) = u4{pkbf(v[0], v[1]), pkbf(v[2], v[3]), pkbf(v[4], v[5]), pkbf(v[6], v[7])};
        }
        return;
    }
    b -= A_KG;
    if (b < A_LN) {
        const int tok = tid >> 4;
        const u2* xr = (const u2*)(A.x + (size_t)(b * 16 + tok) * D_) + i;
        u2 v[9];
        for (int k = 0; k < 9; k++) v[k] = xr[16 * k];
        float s = 0.f;
        for (int k = 0; k < 9; k++) {
            *(u2*)(smem + (i + 16 * k) * 144 + tok * 8) = v[k];
            s += (lo2f(v[k][0]) + hi2f(v[k][0])) + (lo2f(v[k][1]) + hi2f(v[k][1]));
        }
        const float mean = row_sum(s) * (1.f / D_);
        float s2 = 0.f;
        for (int k = 0; k < 9; k++)
            for (int e = 0; e < 2; e++) {
                float d0 = lo2f(v[k][e]) - mean, d1 = hi2f(v[k][e]) - mean;
                s2 += d0 * d0 + d1 * d1;
            }
        const float var = row_sum(s2) * (1.f / D_);
        if (i == 0) *(unsigned*)(A.ws + WS_STAT + (b * 16 + tok) * 4) = pkbf(mean, __builtin_sqrtf(rbf(rbf(var) + 1e-5f)));
        __syncthreads();
        for (int k = 0; k < 5; k++) {
            int g = k * 256 + tid;
            if (g < 1152) st_sc1(A.ws + WS_XQ + (g >> 3) * 16384 + b * 128 + (g & 7) * 16, *(const u4*)(smem + (g >> 3) * 144 + (g & 7) * 16));
        }
        return;
    }
    b -= A_LN;
    if (b < A_MOD) {
        const int cb = b % 54, kp = b / 54, cg = tid & 7, rg = tid >> 3;
        u4 wv[6];
        float sv[6];
        for (int k = 0; k < 6; k++) {
            int row = kp * 192 + k * 32 + rg;
            wv[k] = *(const u4*)(A.wmod + (size_t)row * (6 * D_) + cb * 64 + cg * 8);
            sv[k] = bf2f(A.c[row]);
        }
        float a[8] = {};
        for (int k = 0; k < 6; k++) {
            float s = rbf(sv[k] * __builtin_amdgcn_rcpf(1.f + __builtin_amdgcn_exp2f(-1.44269504f * sv[k])));
            for (int e = 0; e < 4; e++) { a[2 * e] += s * lo2f(wv[k][e]); a[2 * e + 1] += s * hi2f(wv[k][e]); }
        }
        float* red = (float*)smem;
        *(f4*)(red + rg * 68 + cg * 8) = f4{a[0], a[1], a[2], a[3]};
        *(f4*)(red + rg * 68 + cg * 8 + 4) = f4{a[4], a[5], a[6], a[7]};
        __syncthreads();
        const int col = tid >> 2, part = tid & 3;
        float s = 0.f;
        for (int k = 0; k < 8; k++) s += red[(part * 8 + k) * 68 + col];
        s += dppf<0xb1>(s);
        s += dppf<0x4e>(s);
        if (part == 0) ((float*)(A.ws + WS_MODP))[kp * 3456 + cb * 64 + col] = s;
        return;
    }
    b -= A_MOD;
    if (b < A_WD) {
        const int kc = b / 36, kh = (b % 36) / 18, ks = g2_pos(b % 18);
        const u16* r0 = A.wd + (size_t)h_col(b, q, 0) * D_ + wid * 144;
        const u16* r1 = A.wd + (size_t)h_col(b, q, 1) * D_ + wid * 144;
        unsigned v[5][8];
        for (int P = 0; P < 5; P++)
            for (int j = 0; j < 8; j++)
                v[P][j] = *(const unsigned*)((j < 4 ? r0 : r1) + (j & 3) * 6 * D_ + P * 32 + (P < 4 ? 2 * i : (i & 14)));
        char* dst = A.ws + WS_WD + (size_t)(wid * 4 + kc) * STREAM_B + kh * 1024 + lane * 16;
        for (int P = 0; P < 4; P++)
            for (int e = 0; e < 2; e++) {
                u4 o;
                for (int j = 0; j < 4; j++) o[j] = __builtin_amdgcn_perm(v[P][2 * j + 1], v[P][2 * j], e ? 0x07060302u : 0x05040100u);
                st_sc1(dst + g2_slot(kc, 2 * P + e, ks) * 2048, o);
            }
        u4 o;
        const unsigned sel = (i & 1) ? 0x07060302u : 0x05040100u;
        for (int j = 0; j < 4; j++) o[j] = __builtin_amdgcn_perm(v[4][2 * j + 1], v[4][2 * j], sel);
        st_sc1(dst + g2_slot(kc, 8, ks) * 2048, o);
        return;
    }
    for (int k = 0; k < 3; k++) *(u4*)(A.ws + WS_FLAG + (k * 256 + tid) * 16) = u4{0, 0, 0, 0};
}

#define B_CONV 144
#define B_PREP 96
#define B_HP 0
#define B_R  16640
#define B_Y  (B_R + 4 * 9152)
#define B_SMEM (B_Y + 65536)
template <int O, int OB>
__device__ __forceinline__ void cv_rd(u2& al, u2& ah, u4& bf, unsigned aa, unsigned ba) {
    asm volatile("ds_read2_b32 %0, %2 offset0:%3 offset1:%4\n\tds_read2_b32 %1, %2 offset0:%5 offset1:%6"
                 : "=&v"(al), "=&v"(ah) : "v"(aa), "n"(O), "n"(O + 1), "n"(O + 2), "n"(O + 3));
    asm volatile("ds_read_b128 %0, %1 offset:%2" : "=v"(bf) : "v"(ba), "n"(OB));
}
template <int W>
__device__ __forceinline__ void cv_wait(u2& al, u2& ah, u4& bf) {
    asm volatile("s_waitcnt lgkmcnt(%3)" : "+v"(al), "+v"(ah), "+v"(bf) : "n"(W > 15 ? 15 : W));
}
struct BArgs {
    const u16 *dskip, *wg, *wu;
    char* ws;
};
__global__ __launch_bounds__(512, 2) void k_b(BArgs A) {
    extern __shared__ __attribute__((aligned(16))) char smem[];
    int b = blockIdx.x;
    const int tid = threadIdx.x, lane = tid & 63, wid = __builtin_amdgcn_readfirstlane(tid >> 6);
    const int q = lane >> 4, i = lane & 15;
    const float* modp = (const float*)(A.ws + WS_MODP);
    if (b < B_CONV) {
        const int Q = (b >> 4) * 16 + (b & 7) * 2 + ((b >> 3) & 1);
        const unsigned sm = lds_addr(smem);
        const char* ke = A.ws + WS_KR + Q * 4 * 3072;
        glds16(sm + B_R + (wid >> 1) * 9152 + (wid & 1) * 4576 + 3072, lane * 16, ke + (wid >> 1) * 3072 + 1024 + (wid & 1) * 1024);
        const u4* xp = (const u4*)(A.ws + WS_XQ + Q * 16384 + tid * 32);
        const u4 xa = xp[0], xb = xp[1];
        const u4 st = *(const u4*)(A.ws + WS_STAT + tid * 16);
        const unsigned e3 = *(const unsigned*)(ke + (tid >> 7) * 3072 + (tid & 127) * 4);
        const u2 e2 = *(const u2*)(ke + (tid >> 7) * 3072 + 512 + lane * 8);
        float sa[4], c1[4], ga[4], dk[4];
        for (int c = 0; c < 4; c++) {
            const float* m = modp + Q * 4 + c;
            sa[c] = rbf(m[0] + m[3456] + m[6912]);
            c1[c] = rbf(1.f + rbf(m[576] + m[3456 + 576] + m[6912 + 576]));
            ga[c] = rbf(m[1152] + m[3456 + 1152] + m[6912 + 1152]);
            dk[c] = bf2f(A.dskip[Q * 4 + c]);
        }
        if (lane < 28) *(u4*)(smem + B_R + (wid >> 1) * 9152 + (wid & 1) * 4576 + 4096 + lane * 16) = u4{0, 0, 0, 0};
        else if (lane < 32) *(u4*)(smem + B_HP + (wid & 3) * 4160 + (lane - 28) * 16) = u4{0, 0, 0, 0};
        {
            char* r = smem + B_R + (tid >> 7) * 9152;
            const unsigned aa = __builtin_amdgcn_perm(e3, e3, 0x01000100u), bb = __builtin_amdgcn_perm(e3, e3, 0x03020302u);
            const unsigned ba = __builtin_amdgcn_perm(e3, e3, 0x01000302u);
            const bool modd = (tid & 1) == 0;
            *(u4*)(r + (tid & 127) * 16) = u4{bb, modd ? ba : aa, aa, aa};
            *(u4*)(r + 4576 + (tid & 127) * 16) = u4{bb, modd ? bb : ba, aa, aa};
            const unsigned t00 = __builtin_amdgcn_perm(e2[0], e2[0], 0x01000100u), t11 = __builtin_amdgcn_perm(e2[0], e2[0], 0x03020302u);
            const unsigned s0 = (wid & 1) ? 0x05040504u : 0x03020504u, s2 = (wid & 1) ? 0x01000302u : 0x01000100u;
            *(u4*)(r + (wid & 1) * 4576 + 2048 + lane * 16) =
                u4{__builtin_amdgcn_perm(e2[1], e2[0], s0), t11, __builtin_amdgcn_perm(e2[0], e2[0], s2), t00};
        }
        unsigned hd[4][2];
        {
            float hv[4][4];
            for (int t = 0; t < 4; t++) {
                const float m = lo2f(st[t]), rs = __builtin_amdgcn_rcpf(hi2f(st[t]));
                for (int e = 0; e < 2; e++) {
                    const unsigned xw = t < 2 ? xa[2 * t + e] : xb[2 * t - 4 + e];
                    unsigned u = pkbf(lo2f(xw) - m, hi2f(xw) - m);
                    u = pkbf(lo2f(u) * rs, hi2f(u) * rs);
                    u = pkbf(lo2f(u) * c1[2 * e], hi2f(u) * c1[2 * e + 1]);
                    hv[2 * e][t] = lo2f(u) + sa[2 * e];
                    hv[2 * e + 1][t] = hi2f(u) + sa[2 * e + 1];
                }
            }
            for (int c = 0; c < 4; c++) {
                u2 h = {pkbf(hv[c][0], hv[c][1]), pkbf(hv[c][2], hv[c][3])};
                *(u2*)(smem + B_HP + c * 4160 + 64 + tid * 8) = h;
                for (int e = 0; e < 2; e++) hd[c][e] = pkbf(lo2f(h[e]) * dk[c], hi2f(h[e]) * dk[c]);
            }
        }
        wait_vm<0>();
        __syncthreads();
        {
            const int ch = wid >> 1, hf = wid & 1;
            const unsigned ab = sm + B_HP + ch * 4160 + (hf * 64 + 4 * q + i + 1) * 4;
            const unsigned bbase = sm + B_R + ch * 9152 + (i & 1) * 4576 + 448 - 64 * (i >> 1) + 256 * hf + 16 * q;
            f4 acc[8];
            for (int c = 0; c < 8; c++) acc[c] = f4{0.f, 0.f, 0.f, 0.f};
            static_for<0, 4>([&](auto RI) {
                constexpr int rho = decltype(RI)::value;
                u2 al[8], ah[8];
                u4 bf[8];
                static_for<0, 8>([&](auto MI) {
                    constexpr int mu = decltype(MI)::value;
                    constexpr int o = (mu & 1) * 128 + rho * 16;
                    cv_rd<o, (7 - mu) * 512 + rho * 64>(al[mu], ah[mu], bf[mu], ab + (mu >> 1) * 1024, bbase);
                });
                static_for<0, 8>([&](auto CI) {
                    constexpr int n = decltype(CI)::value;
                    constexpr int w = (7 - n) * 3;
                    cv_wait<w>(al[n], ah[n], bf[n]);
                    for (int k = 0; k <= n; k++) {
                        if (n + k < 8) {
                            u4 an = {al[n][0], al[n][1], ah[n][0], ah[n][1]};
                            acc[n + k] = __builtin_amdgcn_mfma_f32_16x16x32_bf16(__builtin_bit_cast(bf8, an), __builtin_bit_cast(bf8, bf[k]), acc[n + k], 0, 0, 0);
                            if (k < n) {
                                u4 ak = {al[k][0], al[k][1], ah[k][0], ah[k][1]};
                                acc[n + k] = __builtin_amdgcn_mfma_f32_16x16x32_bf16(__builtin_bit_cast(bf8, ak), __builtin_bit_cast(bf8, bf[n]), acc[n + k], 0, 0, 0);
                            }
                        }
                    }
                });
            });
            char* yw = smem + B_Y + hf * 32768 + ch * 8192 + (i & 1) * 4096 + (16 * (i >> 1) + 4 * q) * 4;
            for (int c = 0; c < 8; c++) *(f4*)(yw + c * 512) = acc[c];
        }
        __syncthreads();
        float x2[4][4];
        for (int c = 0; c < 4; c++) {
            const char* yr = smem + B_Y + c * 8192 + tid * 8;
            const f2 y0 = *(const f2*)yr + *(const f2*)(yr + 32768), y1 = *(const f2*)(yr + 4096) + *(const f2*)(yr + 32768 + 4096);
            for (int e = 0; e < 2; e++) {
                unsigned u = pkbf(y0[e], y1[e]);
                u = pkbf(lo2f(u) + lo2f(hd[c][e]), hi2f(u) + hi2f(hd[c][e]));
                u = pkbf(lo2f(u) * ga[c], hi2f(u) * ga[c]);
                const unsigned x0 = e ? xb[c >> 1] : xa[c >> 1], x1 = e ? xb[2 + (c >> 1)] : xa[2 + (c >> 1)];
                x2[2 * e][c] = ((c & 1) ? hi2f(x0) : lo2f(x0)) + lo2f(u);
                x2[2 * e + 1][c] = ((c & 1) ? hi2f(x1) : lo2f(x1)) + hi2f(u);
            }
        }
        char* xo = A.ws + WS_X2O + (Q >> 1) * 32768 + tid * 64 + (Q & 1) * 8;
        for (int t = 0; t < 4; t++) *(u2*)(xo + t * 16) = u2{pkbf(x2[t][0], x2[t][1]), pkbf(x2[t][2], x2[t][3])};
        return;
    }
    b -= B_CONV;
    {
        const int up = b / 48, np = (b % 48) / 3, un = b % 3;
        const char* src = (const char*)((up ? A.wu : A.wg) + (size_t)(wid * 32 + q * 8) * F_ + 288 * np + 96 * un + 6 * i);
        typedef __attribute__((ext_vector_type(3))) unsigned u3;
        typedef u3 __attribute__((aligned(4))) u3a;
        u3 v[3][8];
        for (int k = 0; k < 2; k++)
            for (int j = 0; j < 8; j++) v[k][j] = *(const u3a*)(src + (size_t)(k * 256 + j) * (F_ * 2));
        if (wid < 2)
            for (int j = 0; j < 8; j++) v[2][j] = *(const u3a*)(src + (size_t)(512 + j) * (F_ * 2));
        float* tc = (float*)smem;
        float* tsb = tc + 576;
        float* red = tc + 1152;
        if (tid < 288) {
            const f2* m = (const f2*)(modp + 3 * D_) + tid;
            f2 sb = m[0] + m[1728] + m[3456], cb = m[288] + m[1728 + 288] + m[3456 + 288];
            *(f2*)(tc + 2 * tid) = f2{rbf(1.f + rbf(cb[0])), rbf(1.f + rbf(cb[1]))};
            *(f2*)(tsb + 2 * tid) = f2{rbf(sb[0]), rbf(sb[1])};
            if (b == 0) {
                f2 gb = m[576] + m[1728 + 576] + m[3456 + 576];
                *(f2*)(A.ws + WS_MODF + (5 * D_ + 2 * tid) * 4) = f2{rbf(gb[0]), rbf(gb[1])};
            }
        }
        __syncthreads();
        float s1[6] = {}, s2[6] = {};
        auto pass1 = [&](int k) {
            const int k0 = (wid + 8 * k) * 32 + q * 8;
            for (int j = 0; j < 8; j++) {
                const float c = tc[k0 + j], s = tsb[k0 + j];
                for (int e = 0; e < 3; e++) {
                    const float wl = lo2f(v[k][j][e]), wh = hi2f(v[k][j][e]);
                    s1[2 * e] += wl * c; s2[2 * e] += wl * s;
                    s1[2 * e + 1] += wh * c; s2[2 * e + 1] += wh * s;
                }
            }
        };
        pass1(0); pass1(1);
        if (wid < 2) pass1(2);
        {
            float t[6];
            for (int e = 0; e < 3; e++) {
                asm volatile("s_nop 1\n\tv_permlane16_swap_b32 %0, %1\n\tv_permlane16_swap_b32 %2, %3" : "+v"(s1[e]), "+v"(s1[3 + e]), "+v"(s2[e]), "+v"(s2[3 + e]));
                t[e] = s1[e] + s1[3 + e];
                t[3 + e] = s2[e] + s2[3 + e];
            }
            for (int e = 0; e < 3; e++) {
                asm volatile("s_nop 1\n\tv_permlane32_swap_b32 %0, %1" : "+v"(t[e]), "+v"(t[3 + e]));
                red[((3 * q + e) * 16 + i) * 8 + wid] = t[e] + t[3 + e];
            }
        }
        __syncthreads();
        float mean[6];
        for (int c = 0; c < 6; c++) {
            const f4 a = *(const f4*)(red + (c * 16 + i) * 8), bq = *(const f4*)(red + (c * 16 + i) * 8 + 4);
            const f4 s = a + bq;
            mean[c] = ((s[0] + s[1]) + (s[2] + s[3])) * (1.f / D_);
        }
        if (tid < 96) {
            const f4 a = *(const f4*)(red + (96 + tid) * 8), bq = *(const f4*)(red + (96 + tid) * 8 + 4);
            const f4 s = a + bq;
            const int c18 = un * 6 + (tid >> 4);
            *(float*)(A.ws + WS_U12 + (2 * np + c18 / 9) * 2048 + ((c18 % 9) * 32 + up * 16 + (tid & 15)) * 4) = (s[0] + s[1]) + (s[2] + s[3]);
        }
        auto pass2 = [&](int k) {
            const int ks = wid + 8 * k, k0 = ks * 32 + q * 8;
            float c[8];
            for (int j = 0; j < 8; j++) { c[j] = tc[k0 + j]; asm volatile("" : "+v"(v[k][j])); }
            for (int cc = 0; cc < 6; cc++) {
                u4 o;
                for (int j = 0; j < 4; j++) {
                    const unsigned w0 = v[k][2 * j][cc >> 1], w1 = v[k][2 * j + 1][cc >> 1];
                    o[j] = pkbf(((cc & 1) ? hi2f(w0) : lo2f(w0)) * c[2 * j] - mean[cc], ((cc & 1) ? hi2f(w1) : lo2f(w1)) * c[2 * j + 1] - mean[cc]);
                }
                const int c18 = un * 6 + cc, nt = 2 * np + c18 / 9, ch = c18 % 9;
                const int fi = ch < 4 ? ks * 8 + ch * 2 + up : ch < 8 ? (ch >> 1) * 72 + ks * 4 + (ch & 1) * 2 + up : 288 + ks * 2 + up;
                st_sc1(A.ws + WS_W1 + (size_t)nt * STREAM_B + fi * 1024 + lane * 16, o);
            }
        };
        pass2(0); pass2(1);
        if (wid < 2) pass2(2);
    }
}

#if defined(__HIP_DEVICE_COMPILE__)
#define NOPK __attribute__((target("no-packed-fp32-ops")))
#else
#define NOPK
#endif
#define G_WT 32
#define G_WU 32
#define G_WU2 56
#define G_HS 16
#define G_TZ0 44
#define G_TZS 24
#define G_LK 3
#define G_HE 6
#define G_TSL -16
#define G_TST 12
#define G_UE 23
#define G_PACE 5
#define G_URG 36
#define G_TL 150
#define G_PRIO 1
#define G_LEADHI 1
namespace gf {
constexpr int NS1 = 324, NS = 486, LAG = 32, NU = 82, RING = 19, WT = G_WT, WU = G_WU, TSD = 110;
constexpr int NSYNC = 26, LK = G_LK;
constexpr int PP[2][NSYNC + 1] = {
    {0, 8, 16, 32, 48, 64, 80, 104, 128, 176, 192, 208, 232, 256, 280, 304, 322, 340, 356, 372, 392, 412, 428, 440, 452, 468, 486},
    {-32, -24, -16, 0, 16, 32, 48, 72, 96, 144, 168, 192, 216, 240, 264, 288, 308, 324, 348, 372, 392, 412, 428, 440, 452, 468, 486}};
constexpr int JFLAG[4] = {12, 14, 16, 18};
constexpr int TPL[2][4] = {{248, 300, 342, 374}, {234, 292, 330, 374}};
constexpr int TPC[2][4] = {{260, 310, 352, 384}, {246, 302, 340, 384}};
constexpr int TEP[4] = {143, 215, 287, 323};
constexpr int THS[9] = {143, 143 + G_HS, 143 + 2 * G_HS, 143 + 3 * G_HS, 215, 215 + G_HS, 287, 287, 323};
constexpr int TZ0 = G_TZ0, TZS = G_TZS, TSTAT = G_TST;
constexpr int isflag(int j) { return j == JFLAG[0] ? 0 : j == JFLAG[1] ? 1 : j == JFLAG[2] ? 2 : j == JFLAG[3] ? 3 : -1; }
constexpr int TX[3] = {NS1 + 104, NS1 + 116, NS1 + 128}, TMISC = NS1 + 134, TL = NS1 + G_TL;
constexpr int hneed(int ks) { return NS1 + (ks < 12 ? ks * 7 : 48 + ks * 3); }
constexpr int ufirst(int u) { return u < 41 ? 8 * u : NS1 + 4 * (u - 41); }
constexpr int ulast(int u) { return u < 40 ? 8 * u + 7 : u == 40 ? NS1 - 1 : NS1 + 4 * (u - 41) + 3; }
constexpr int page(int u) { int r = u < 80 ? (u + 11) % RING : u - 80; return r < 15 ? r : r + 1; }
constexpr int prio(int r, int t) {
    int j = 0;
    while (PP[r][j + 1] <= t) j++;
    int e = -1;
    for (int g = 0; g < 4; g++)
        if (PP[0][j] <= TEP[g] && TEP[g] < PP[0][j + 1]) e = TEP[g];
    if (e >= 0) return r ? (t == PP[r][j] ? 2 : -1) : t == PP[r][j] ? 3 : t == e + 1 ? 0 : -1;
    int n = PP[r][j + 1] - PP[r][j], a = (t - PP[r][j]) * 4 / n;
    return t == PP[r][j] || a != (t - 1 - PP[r][j]) * 4 / n ? 3 - a : -1;
}
constexpr int NT = NS + LAG;
struct Sched {
    int nop;
    int op[200];
    int lo[NT + 1];
    int sync_at[NT];
    int sync_wait[NSYNC];
    int tok_wait[18], h_wait[18], poll_wait[4];
    int stat_wait, sd_wait, minlead;
    bool ok;
};
constexpr Sched make(int r) {
    Sched s{};
    s.ok = true;
    s.minlead = 1000;
    const int* P = PP[r];
    const int t0 = P[0];
    int est[320] = {}, need[320] = {}, pos[320] = {}, jv[NU] = {};
    bool live[320] = {};
    for (int u = 0; u < NU; u++) {
        int j = 0;
        while (ufirst(u) > PP[0][j + 1] + LK - 1) j++;
        jv[u] = j;
        need[u] = P[j];
        est[u] = u < G_UE ? P[j] - WU : u < 41 ? P[j] - G_WU2 : 0;
        if (u >= RING) {
            int jf = 0;
            while (PP[1][jf] <= ulast(u < 80 ? u - RING : u - 15)) jf++;
            if (jf >= j) s.ok = false;
            if (est[u] < P[jf]) est[u] = P[jf];
        }
        live[u] = true;
    }
    for (int k = 0; k < 36; k++) {
        need[100 + k] = (k >> 1) * 8;
        est[100 + k] = need[100 + k] - WT + (k & 1) * 4;
        need[200 + k] = hneed(k >> 1);
        int e = k < G_HE ? 0 : 290 + ((k - G_HE) & ~1), c = TPC[r][k < 16 ? 0 : k < 24 ? 1 : k < 32 ? 2 : 3];
        est[200 + k] = e > c ? e : c;
        live[100 + k] = live[200 + k] = true;
    }
    if (r == 1)
        for (int k = 300; k < 304; k++) { need[k] = TSTAT; est[k] = G_TSL; live[k] = true; }
    int n = 0, last = -1000, j = 0, pos_sd = 0, pos_st[4] = {}, pos_fl = 0;
    for (int t = t0 - 1; t < NS; t++) {
        if (t >= t0) {
            s.lo[t - t0] = s.nop;
            s.sync_at[t - t0] = -1;
            if (P[j] == t) {
                s.sync_at[t - t0] = j;
                int m = -1;
                for (int u = 0; u < NU; u++)
                    if (jv[u] <= j) { if (live[u]) s.ok = false; if (pos[u] > m) m = pos[u]; }
                if (r == 1 && j >= 6 && pos[300] > m) m = pos[300];
                int g = isflag(j);
                if (g >= 0 && pos_st[g] > m) m = pos_st[g];
                if (g >= 0 && t <= THS[g == 0 ? 3 : g == 3 ? 8 : 2 * g + 3]) s.ok = false;
                s.sync_wait[j] = n - 1 - m;
                j++;
            }
            for (int g = 0; g < 4; g++)
                if (t == TPC[r][g]) s.poll_wait[g] = n - 1 - pos_fl;
        }
        while (true) {
            int b = -1;
            for (int o = 0; o < 304; o++)
                if (live[o] && est[o] <= t && (b < 0 || need[o] < need[b])) b = o;
            if (b < 0) break;
            if (t >= 0 && need[b] - t > G_URG && 2 * t - last < G_PACE) break;
            live[b] = false;
            pos[b] = n++;
            s.op[s.nop++] = b;
            last = 2 * t - last < G_PACE + 2 && 2 * t - last >= G_PACE ? last + G_PACE : 2 * t;
            if (t >= 40 && need[b] - t < s.minlead) s.minlead = need[b] - t;
            if (t >= 0) break;
        }
        if (r == 1 && t == TSTAT) { if (live[303]) s.ok = false; s.stat_wait = n - 1 - pos[303]; n++; }
        if (t >= 0 && t < 144 && t % 8 == 0) {
            int a = 100 + t / 4;
            if (live[a] || live[a + 1]) s.ok = false;
            s.tok_wait[t / 8] = n - 1 - (pos[a] > pos[a + 1] ? pos[a] : pos[a + 1]);
        }
        for (int ks = 0; ks < 18; ks++)
            if (t == hneed(ks)) {
                int a = 200 + ks * 2;
                if (live[a] || live[a + 1]) s.ok = false;
                s.h_wait[ks] = n - 1 - (pos[a] > pos[a + 1] ? pos[a] : pos[a + 1]);
            }
        if (t == TSD) { n += 2; pos_sd = n - 1; }
        if (t == 143) s.sd_wait = n - 1 - pos_sd;
        for (int i = 0; i < 9; i++)
            if (t == THS[i]) pos_st[i < 4 ? 0 : i / 2 - 1] = n++;
        if (t >= TZ0 && t < TZ0 + 4 * TZS && (t - TZ0) % TZS == 0) n++;
        for (int g = 0; g < 4; g++)
            if (t == TPL[r][g]) pos_fl = n++;
        if (t == TX[0] + 2 || t == TX[0] + 4 || t == TX[1] + 3 || t == TX[2] + 3 || (t >= TMISC && t < TMISC + 5)) n++;
    }
    s.lo[NS - t0] = s.nop;
    if (j != NSYNC || TL < P[NSYNC - 1] || TZ0 + 3 * TZS >= 143) s.ok = false;
    return s;
}
template <int R> struct SS { static constexpr Sched s = make(R); };
static_assert(SS<0>::s.ok && SS<1>::s.ok, "bad schedule");
constexpr int TAB_OFF = 15 * 8192, XA_OFF = 15 * 8192, XB_OFF = 18 * 8192, SMEM = 20 * 8192;

template <int N>
__device__ __forceinline__ void wait_vm_reg2(u4& a, u4& b) {
    asm volatile("s_waitcnt vmcnt(%2)" : "+v"(a), "+v"(b) : "n"(N > 63 ? 63 : N));
}
__device__ __forceinline__ float half_sum(float v) {
    v = row_sum(v);
    float w = v;
    asm volatile("s_nop 1\n\tv_permlane16_swap_b32 %0, %1\n\ts_nop 1" : "+v"(v), "+v"(w));
    return v + w;
}
typedef __attribute__((address_space(3))) f4* lds_f4w;
__device__ __forceinline__ void gload_sc1(u4& r, unsigned voff, const char* base) {
    asm volatile("global_load_dwordx4 %0, %1, %2 sc1" : "=v"(r) : "v"(voff), "s"(base));
}
__device__ __forceinline__ void st16(u4 o, unsigned voff, char* base) {
    asm volatile("global_store_dwordx4 %0, %1, %2 sc1\n\ts_nop 1" ::"v"(voff), "v"(o), "s"(base) : "memory");
}
template <int HI>
__device__ __forceinline__ void st4(unsigned o, unsigned voff, char* base) {
    asm volatile("s_mov_b32 exec_lo, 1\n\ts_mov_b32 exec_hi, %3\n\tglobal_store_dword %0, %1, %2 sc1\n\ts_mov_b64 exec, -1" ::"v"(voff), "v"(o), "s"(base), "n"(HI) : "memory");
}
__device__ __forceinline__ void ld4(unsigned& o, unsigned voff, const char* base) {
    asm volatile("global_load_dword %0, %1, %2 sc1" : "=v"(o) : "v"(voff), "s"(base));
}
__device__ __forceinline__ void pub(u4 o, unsigned voff, char* base) {
    o[0] = o[0] > 1u ? o[0] : 1u;
    o[2] = o[2] > 1u ? o[2] : 1u;
    st16(o, voff, base);
}
template <int O>
__device__ __forceinline__ void st16o(u4 o, unsigned voff, char* base) {
    asm volatile("global_store_dwordx4 %0, %1, %2 offset:%3 sc1\n\ts_nop 1" ::"v"(voff), "v"(o), "s"(base), "n"(O) : "memory");
}
__device__ __forceinline__ void swap2(u4& a, u4& b) {
    unsigned a2 = a[2], a3 = a[3], b0 = b[0], b1 = b[1];
    asm volatile("v_swap_b32 %0, %2\n\tv_swap_b32 %1, %3\n\ts_nop 1" : "+v"(a2), "+v"(a3), "+v"(b0), "+v"(b1));
    a[2] = a2; a[3] = a3; b[0] = b0; b[1] = b1;
}
template <int N>
__device__ __forceinline__ void poll(unsigned& fl, unsigned voff, const char* base, unsigned long long mask) {
    asm volatile(
        "s_waitcnt vmcnt(%4)\n"
        "1:\n\t"
        "v_cmp_eq_u32 vcc, 0, %0\n\t"
        "s_nop 1\n\t"
        "s_and_b64 vcc, vcc, %3\n\t"
        "s_cbranch_scc0 2f\n\t"
        "s_sleep 1\n\t"
        "global_load_dword %0, %1, %2 sc1\n\t"
        "s_waitcnt vmcnt(0)\n\t"
        "s_branch 1b\n"
        "2:"
        : "+v"(fl)
        : "v"(voff), "s"(base), "s"(mask), "n"(N > 63 ? 63 : N)
        : "vcc", "scc", "memory");
}

template <int R>
__device__ __forceinline__ void body(unsigned sm, char* ws, char* out, int wid, int lane) {
    using S = SS<R>;
    constexpr int T0 = PP[R][0];
    TSDECL(1);
    TSR(0); TS(1);
    const int b = blockIdx.x, xcd = b & 7, idx = b >> 3;
    const int mt = (xcd & 1) * 4 + (idx & 3), nt = (xcd >> 1) * 8 + (idx >> 2);
    const int p = xcd >> 1, mt2 = (xcd & 1) * 8 + (idx & 7), nt2 = idx >> 3;
    const int tk = (wid & 3) * 2 + R;
    unsigned lb0 = sm + lane * 16;
    asm volatile("" : "+v"(lb0));
    char* wb = ws - sm;
    const char* xsrc = ws + WS_X2O;
    const unsigned voff = ((lane >> 4) * 2048 + mt * 256 + wid * 32 + (lane & 15)) * 16;
    const char* wsrc = wb + WS_W1 + (size_t)nt * STREAM_B + wid * 1024;
    const char* wsrc2 = wb + WS_WD + (size_t)(nt2 * 4 + p) * STREAM_B + wid * 1024;
    char* hdst = wb + WS_H + ((size_t)((mt * 16 + wid * 2) * 144 + (nt >> 1) * 9) * 64) * 16 + (nt & 1) * 4096;
    char* hdst1 = hdst + 147456;
    const char* h0 = wb + WS_H + (size_t)((mt2 * 8 + tk) * 144 + p * 36 + R * 18) * 1024;
    const char* h1 = h0 + (R ? -147456 : 147456);
    char* sdp = ws + WS_SD + mt * 1024;
    char* part = wb + WS_PART + (size_t)(mt2 * 4 + nt2) * (128 * 1024);
    const char* flp = ws + WS_FLAG + (mt2 >> 1) * 512;
    const unsigned wdst = sm + wid * 1024;
    u4 tok[36], hk[36], st[3];
    auto issue1 = [&](auto I) {
        constexpr int op = S::s.op[decltype(I)::value];
        if constexpr (op < 41) glds16(wdst + page(op) * 8192, lb0, wsrc + op * 8192);
        else if constexpr (op < 100) glds16(wdst + page(op) * 8192, lb0, wsrc2 + (op - 41) * 8192);
        else if constexpr (op < 200) gload16(tok[op - 100], voff, xsrc + ((op - 100) >> 1) * 131072 + ((op - 100) & 1) * 256);
        else if constexpr (op < 300) gload_sc1(hk[op - 200], lb0, ((op & 1) ? h1 : h0) + g2_inv((op - 200) >> 1) * 1024);
        else if constexpr (op == 300) glds16(sm + TAB_OFF + (wid & 1) * 1024, lb0, wb + WS_U12 + nt * 2048 + (wid & 1) * 1024);
        else gload16(st[op - 301], (lane & (op == 303 ? 7 : 31)) * 32768 + (lane >> 5) * 16,
                     xsrc + (mt * 256 + nt * 8 + (wid & 3) * 2) * 16 + (op - 301) * 32 * 32768);
    };
    static_for<0, S::s.lo[0]>(issue1);
    TS(2);
    unsigned lbc = 0;
    auto frag = [&](auto FI) -> u4 {
        constexpr int f = decltype(FI)::value;
        constexpr auto foff = [](int f) { return f < NS1 ? page(f / 8) * 8192 + (f % 8) * 1024 : page(41 + (f - NS1) / 4) * 8192 + ((f - NS1) % 4) * 2048 + R * 1024; };
        constexpr int o = foff(f);
        if constexpr (f == 0 || (foff(f - 1) >> 16) != (o >> 16)) {
            if constexpr (o >= 65536) { lbc = lb0 + (o & ~65535); asm volatile("" : "+v"(lbc)); }
        }
        return o < 65536 ? ldsr(lb0 + o) : ldsr(lbc + (o & 65535));
    };
    f4 acc[8][2], aco[2][2], xv[7], gb0, gb1;
    u4 fr[LK + 1], x2v, hs[4], pl[4];
    float rs[2] = {0.f, 0.f};
    unsigned sdv[2], fl, x2c, gbc, xw = 0, xr = 0, off8 = 0;
    int trow = 0;
    const char* gbp = ws + WS_MODF + 5 * D_ * 4;
    static_for<T0, NS>([&](auto TI) {
        constexpr int t = decltype(TI)::value;
        if constexpr (S::s.sync_at[t - T0] >= 0) {
            constexpr int j = S::s.sync_at[t - T0];
            __builtin_amdgcn_sched_barrier(0);
            if constexpr (t == TX[0] || t == TX[1] || t == TX[2]) asm volatile("s_waitcnt lgkmcnt(0)" ::: "memory");
            wait_vm<S::s.sync_wait[j]>();
            __builtin_amdgcn_s_barrier();
            asm volatile("" ::: "memory");
            TS(3 + j);
            if constexpr (R == 0 && isflag(j) >= 0)
                if ((wid & 3) == 0) st4<0>(1u, 0u, ws + WS_FLAG + mt * 512 + nt * 4 + isflag(j) * 128);
            __builtin_amdgcn_sched_barrier(0);
        }
        if constexpr (G_PRIO && t >= 0 && prio(R, t) >= 0) __builtin_amdgcn_s_setprio(prio(R, t));
        if constexpr (t == TPC[R][0]) poll<S::s.poll_wait[0]>(fl, (lb0 - sm) >> 2, flp, 15ull << (p * 8 + R * 4));
        if constexpr (t == TPC[R][1]) poll<S::s.poll_wait[1]>(fl, (lb0 - sm) >> 2, flp, (15ull << (32 + p * 8 + R * 4)) | 0xffffffffull);
        if constexpr (t == TPC[R][2]) poll<S::s.poll_wait[2]>(fl, (lb0 - sm) >> 2, flp + 256, 15ull << (p * 8 + R * 4));
        if constexpr (t == TPC[R][3]) poll<S::s.poll_wait[3]>(fl, (lb0 - sm) >> 2, flp + 256, 15ull << (32 + p * 8 + R * 4));
        static_for<S::s.lo[t - T0], S::s.lo[t - T0 + 1]>(issue1);
        if constexpr (R == 1 && t == TSTAT) {
            asm volatile("s_waitcnt vmcnt(%3)" : "+v"(st[0]), "+v"(st[1]), "+v"(st[2]) : "n"(S::s.stat_wait));
            const bool x = (lane & 31) < 8;
            float v[24];
            for (int i = 0; i < 12; i++) {
                unsigned a = st[i >> 2][i & 3];
                if (i >= 8) a = x ? a : 0u;
                v[2 * i] = lo2f(a);
                v[2 * i + 1] = hi2f(a);
            }
            float a0 = 0.f, a1 = 0.f;
            for (int i = 0; i < 12; i++) { a0 += v[i]; a1 += v[12 + i]; }
            const float mean = half_sum(a0 + a1) * (1.f / D_);
            float q0 = 0.f, q1 = 0.f, q2 = 0.f;
            for (int i = 0; i < 8; i++) {
                float d0 = v[i] - mean, d1 = v[8 + i] - mean, d2 = v[16 + i] - mean;
                q0 += d0 * d0; q1 += d1 * d1; q2 += d2 * d2;
            }
            const float var = half_sum(q0 + q1 + (x ? q2 : 0.f)) * (1.f / D_);
            st4<1>(((unsigned)f2bf(__builtin_sqrtf(rbf(rbf(var) + 1e-5f))) << 16) | 1u, (lane >> 5) * 4, sdp + (nt * 8 + (wid & 3) * 2) * 4);
            __builtin_amdgcn_sched_barrier(0);
        }
        if constexpr (t >= 0) {
            if constexpr (t == 0) static_for<0, LK>([&](auto FI) { fr[decltype(FI)::value] = frag(FI); });
            constexpr int t2 = t - NS1;
            constexpr int ks = t < 144 ? t / 8 : t < 288 ? (t % 72) / 4 : t < NS1 ? (t - 288) / 2 : t2 < 84 ? t2 / 7 : t2 < 102 ? (t2 - 48) / 3 : t2 < 126 ? 12 + (t2 - 102) % 12 / 2 : (t2 - 126) / 2;
            constexpr int a = t < 144 ? t % 8 : t < 288 ? t % 4 : (t < NS1 || t2 >= 126) ? t % 2 : t2 < 84 ? t2 % 7 : t2 < 102 ? t2 % 3 : (t2 < 114 ? 3 : 5) + t % 2;
            if constexpr (t < 144 && a == 0) wait_vm_reg2<S::s.tok_wait[ks]>(tok[2 * ks], tok[2 * ks + 1]);
            if constexpr (t == hneed(ks)) {
                wait_vm_reg2<S::s.h_wait[ks]>(hk[2 * ks], hk[2 * ks + 1]);
                if constexpr (ks >= 16) swap2(hk[2 * ks + R], hk[2 * ks + 1 - R]);
            }
            if constexpr (t + LK < NS) fr[(t + LK) % (LK + 1)] = frag(std::integral_constant<int, t + LK>{});
            for (int sub = 0; sub < 2; sub++) {
                f4& c = t2 >= 126 ? aco[a][sub] : acc[a][sub];
                const u4& bo = t2 >= 0 ? hk[2 * ks + sub] : tok[2 * ks + sub];
                if constexpr (ks == 0) mfma0(c, fr[t % (LK + 1)], bo);
                else mfma(c, fr[t % (LK + 1)], bo);
                __builtin_amdgcn_sched_barrier(0);
            }
            if constexpr (t == TSD) {
                unsigned o = (lb0 - sm) >> 2 & 0x3c;
                asm volatile("global_load_dword %0, %2, %3 sc1\n\tglobal_load_dword %1, %2, %3 offset:64 sc1"
                             : "=&v"(sdv[0]), "=&v"(sdv[1]) : "v"(o), "s"(sdp + wid * 128));
            }
            if constexpr (t == 143) {
                unsigned o = (lb0 - sm) >> 2 & 0x3c, tmp;
                asm volatile(
                    "s_waitcnt vmcnt(%5)\n"
                    "1:\n\t"
                    "v_and_b32 %2, %0, %1\n\t"
                    "v_and_b32 %2, 1, %2\n\t"
                    "v_cmp_eq_u32 vcc, 0, %2\n\t"
                    "s_cbranch_vccz 2f\n\t"
                    "s_sleep 2\n\t"
                    "global_load_dword %0, %3, %4 sc1\n\t"
                    "global_load_dword %1, %3, %4 offset:64 sc1\n\t"
                    "s_waitcnt vmcnt(0)\n\t"
                    "s_branch 1b\n"
                    "2:"
                    : "+v"(sdv[0]), "+v"(sdv[1]), "=&v"(tmp)
                    : "v"(o), "s"(sdp + wid * 128), "n"(S::s.sd_wait)
                    : "vcc");
                for (int sub = 0; sub < 2; sub++) {
                    unsigned bb = sdv[sub] & 0xffff0000u;
                    rs[sub] = __builtin_amdgcn_rcpf(__builtin_bit_cast(float, bb));
                }
            }
            if constexpr (t == TEP[0] || t == TEP[1] || t == TEP[2] || t == TEP[3]) {
                constexpr int g = t / 72 - 1;
                __builtin_amdgcn_sched_barrier(0);
                TS(29 + 2 * g);
                asm volatile("s_nop 7");
                constexpr int NC = g == 0 ? 4 : g < 3 ? 2 : 1, C0 = g == 0 ? 0 : 2 * g + 2;
                unsigned tb = sm + TAB_OFF + (((lb0 - sm) >> 8) << 4);
                asm volatile("" : "+v"(tb));
                f4 ta = ldsrf(tb + C0 * 128), tc = ldsrf(tb + C0 * 128 + 64);
                static_for<0, NC>([&](auto CI) {
                    constexpr int c = decltype(CI)::value;
                    f4 ua = ta, uc = tc;
                    if constexpr (c + 1 < NC) {
                        ta = ldsrf(tb + (C0 + c + 1) * 128);
                        tc = ldsrf(tb + (C0 + c + 1) * 128 + 64);
                    }
                    for (int sub = 0; sub < 2; sub++) {
                        f4 pr = (acc[2 * c][sub] * rs[sub] + ua) * (acc[2 * c + 1][sub] * rs[sub] + uc);
                        u4& o = hs[g < 3 ? (c & 2) + sub : 0];
                        const int h = g < 3 ? (c & 1) * 2 : sub * 2;
                        o[h] = pkbf(pr[0], pr[1]);
                        o[h + 1] = pkbf(pr[2], pr[3]);
                    }
                    __builtin_amdgcn_sched_barrier(0);
                });
                TS(30 + 2 * g);
            }
            static_for<0, 8>([&](auto II) {
                constexpr int i = decltype(II)::value;
                if constexpr (t == THS[i]) st16o<(i >> 1) * 1024>(hs[i < 4 ? i : i & 1], lb0, (i & 1) ? hdst1 : hdst);
            });
            if constexpr (t == THS[8]) st16(hs[0], lb0, hdst + 8192 + (nt & 1) * (147456 - 4096));
            if constexpr (t >= TZ0 && t < TZ0 + 4 * TZS && (t - TZ0) % TZS == 0)
                st16(u4{0, 0, 0, 0}, lb0, wb + WS_PART + (size_t)(mt * 32 + nt) * 32768 + ((t - TZ0) / TZS * 8 + wid) * 1024);
            if constexpr (t == TPL[R][0] || t == TPL[R][1]) ld4(fl, (lb0 - sm) >> 2, flp);
            if constexpr (t == TPL[R][2] || t == TPL[R][3]) ld4(fl, (lb0 - sm) >> 2, flp + 256);
            if constexpr (t == TX[0] - 3) {
                xw = lb0 + XA_OFF + wid * 1024;
                xr = lb0 + XA_OFF + (wid ^ 4) * 1024;
            }
            static_for<0, 7>([&](auto CI) {
                constexpr int c = decltype(CI)::value, g = c < 3 ? 0 : (c - 1) >> 1, i = c < 3 ? c : (c - 1) & 1;
                constexpr int o = (g == 1 ? XB_OFF - XA_OFF : 0) + i * 8192;
                if constexpr (t == TX[g] - (g ? 2 : 3) + i) *(lds_f4w)(size_t)(xw + o) = acc[c][1];
                if constexpr (t == TX[g] + i) xv[c] = ldsrf(xr + o);
                if constexpr (c == 0 && t == TX[0] + 2) pub(__builtin_bit_cast(u4, acc[0][0] + xv[0]), lb0, part + 96 * 1024 + (p * 8 + tk) * 1024);
                if constexpr ((c & 1) && t == TX[g] + (g ? 3 : 4)) {
                    const f4 va = acc[c][0] + xv[c], vb = acc[c + 1][0] + xv[c + 1];
                    pub(u4{pkbf(va[0], vb[0]), pkbf(va[1], vb[1]), pkbf(va[2], vb[2]), pkbf(va[3], vb[3])}, lb0,
                        part + ((((p + 1 + g) & 3) * 3 + g) * 8 + tk) * 1024);
                }
            });
            if constexpr (t == TMISC) {
                const int q = lane >> 4, tl = lane & 15;
                trow = (mt2 * 8 + tk) * 16 + tl;
                off8 = sm + 96 * 1024 + (q * 8 + tk) * 1024 + (p * 16 + tl) * 16;
                gload16(x2v, ((nt2 * 18 + p * 4 + q) * 2048 + trow) * 16, xsrc);
            }
            if constexpr (t == TMISC + 1)
                asm volatile("global_load_ushort %0, %1, %2" : "=v"(x2c) : "v"(((nt2 * 18 + 16 + (p >> 1)) * 2048 + trow) * 16 + ((p & 1) * 4 + (lane >> 4)) * 2), "s"(xsrc));
            if constexpr (t == TMISC + 2) asm volatile("global_load_dwordx4 %0, %1, %2" : "=v"(gb0) : "v"((lane >> 4) * 32), "s"(gbp + (nt2 * 144 + p * 32) * 4));
            if constexpr (t == TMISC + 3) asm volatile("global_load_dwordx4 %0, %1, %2 offset:16" : "=v"(gb1) : "v"((lane >> 4) * 32), "s"(gbp + (nt2 * 144 + p * 32) * 4));
            if constexpr (t == TMISC + 4) asm volatile("global_load_dword %0, %1, %2" : "=v"(gbc) : "v"((lane >> 4) * 4), "s"(gbp + (nt2 * 144 + 128 + p * 4) * 4));
            if constexpr (t == TL) gload_sc1(pl[3], off8, part);
            if constexpr (t > TL && t < TL + 4) gload_sc1(pl[t - TL - 1], lb0, part + ((p * 3 + t - TL - 1) * 8 + tk) * 1024);
            __builtin_amdgcn_sched_barrier(0);
        }
    });
    TS(37);
    asm volatile("s_nop 7");
    *(lds_f4w)(size_t)(xw + XB_OFF - XA_OFF) = aco[0][1];
    *(lds_f4w)(size_t)(xw + XB_OFF - XA_OFF + 8192) = aco[1][1];
    asm volatile("s_waitcnt lgkmcnt(0)" ::: "memory");
    __builtin_amdgcn_s_barrier();
    asm volatile("" ::: "memory");
    f4 o0 = aco[0][0] + ldsrf(xr + XB_OFF - XA_OFF), o1 = aco[1][0] + ldsrf(xr + XB_OFF - XA_OFF + 8192);
    TS(38);
    asm volatile("s_waitcnt vmcnt(0)" : "+v"(pl[0]), "+v"(pl[1]), "+v"(pl[2]), "+v"(pl[3]), "+v"(x2v), "+v"(gb0), "+v"(gb1), "+v"(x2c), "+v"(gbc));
    TS(39);
    while (true) {
        unsigned tg = 1u;
        for (int i = 0; i < 4; i++) tg = min(tg, min(pl[i][0], pl[i][2]));
        if (__builtin_amdgcn_ballot_w64(tg == 0) == 0) break;
        __builtin_amdgcn_s_sleep(1);
        for (int i = 0; i < 3; i++) gload_sc1(pl[i], lb0, part + ((p * 3 + i) * 8 + tk) * 1024);
        gload_sc1(pl[3], off8, part);
        asm volatile("s_waitcnt vmcnt(0)" : "+v"(pl[0]), "+v"(pl[1]), "+v"(pl[2]), "+v"(pl[3]));
    }
    TS(40);
    const int q = lane >> 4;
    for (int r = 0; r < 4; r++) {
        o0[r] += (lo2f(pl[0][r]) + lo2f(pl[1][r])) + lo2f(pl[2][r]);
        o1[r] += (hi2f(pl[0][r]) + hi2f(pl[1][r])) + hi2f(pl[2][r]);
    }
    u4 ov;
    for (int r = 0; r < 4; r++) {
        unsigned ob = pkbf(o0[r], o1[r]);
        float ga = r < 2 ? gb0[2 * r] : gb1[2 * r - 4], gc = r < 2 ? gb0[2 * r + 1] : gb1[2 * r - 3];
        unsigned pb = pkbf(ga * lo2f(ob), gc * hi2f(ob));
        ov[r] = pkbf(lo2f(x2v[r]) + lo2f(pb), hi2f(x2v[r]) + hi2f(pb));
    }
    *(u4*)(out + (size_t)trow * 1152 + (nt2 * 144 + p * 32 + q * 8) * 2) = ov;
    {
        const f4 p8 = __builtin_bit_cast(f4, pl[3]);
        float u0 = p8[0], u1 = p8[1], u2 = p8[2], u3 = p8[3];
        asm volatile("s_nop 1\n\tv_permlane16_swap_b32 %0, %1\n\tv_permlane16_swap_b32 %2, %3" : "+v"(u0), "+v"(u1), "+v"(u2), "+v"(u3));
        float a = u0 + u1, c = u2 + u3;
        asm volatile("s_nop 1\n\tv_permlane32_swap_b32 %0, %1" : "+v"(a), "+v"(c));
        float o = a + c;
        *(u16*)(out + (size_t)trow * 1152 + (nt2 * 144 + 128 + p * 4 + q) * 2) = f2bf(lo2f(x2c) + rbf(__builtin_bit_cast(float, gbc) * rbf(o)));
    }
    TSFLUSH(43);
}
}

__global__ NOPK __launch_bounds__(512, 2) void k_g(char* ws, char* out) {
    extern __shared__ __attribute__((aligned(16))) char smem[];
    const unsigned sm = lds_addr(smem);
    const int tid = threadIdx.x, lane = tid & 63;
    const int wid = __builtin_amdgcn_readfirstlane(tid >> 6);
    if ((wid < 4) != G_LEADHI) {
        gf::body<0>(sm, ws, out, wid, lane);
        __builtin_amdgcn_endpgm();
    }
    gf::body<1>(sm, ws, out, wid, lane);
}

extern "C" void kernel_launch(void* const* d_in, const int* in_sizes, int n_in,
                              void* d_out, int out_size, void* d_ws, size_t ws_size,
                              hipStream_t stream) {
    char* ws = (char*)d_ws;
    AArgs a;
    a.x = (const u16*)d_in[0];
    a.Are = (const u16*)d_in[1];
    a.Aim = (const u16*)d_in[2];
    a.Bre = (const u16*)d_in[3];
    a.Bim = (const u16*)d_in[4];
    a.Cre = (const u16*)d_in[5];
    a.Cim = (const u16*)d_in[6];
    a.logdt = (const u16*)d_in[7];
    a.c = (const u16*)d_in[13];
    a.wmod = (const u16*)d_in[14];
    a.wd = (const u16*)d_in[12];
    a.ws = ws;
    BArgs bb;
    bb.dskip = (const u16*)d_in[8];
    bb.wg = (const u16*)d_in[10];
    bb.wu = (const u16*)d_in[11];
    bb.ws = ws;
    (void)hipFuncSetAttribute((const void*)k_b, hipFuncAttributeMaxDynamicSharedMemorySize, B_SMEM);
    (void)hipFuncSetAttribute((const void*)k_g, hipFuncAttributeMaxDynamicSharedMemorySize, gf::SMEM);
    k_a<<<A_KG + A_LN + A_MOD + A_WD + A_ZERO, 256, 0, stream>>>(a);
    k_b<<<B_CONV + B_PREP, 512, B_SMEM, stream>>>(bb);
    k_g<<<256, 512, gf::SMEM, stream>>>(ws, (char*)d_out);
}
```

```cpp
#include <hip/hip_runtime.h>
#include <hip/hip_bf16.h>
#include <stdint.h>
#include <type_traits>
#include <utility>

typedef unsigned short u16;
typedef __attribute__((ext_vector_type(8))) __bf16 bf8;
typedef __attribute__((ext_vector_type(2))) __bf16 bf2;
typedef __attribute__((ext_vector_type(2))) float f2;
typedef __attribute__((ext_vector_type(4))) float f4;
typedef __attribute__((ext_vector_type(16))) float f16v;
typedef __attribute__((ext_vector_type(4))) unsigned u4;
typedef __attribute__((ext_vector_type(2))) unsigned u2;

#define L_ 2048
#define D_ 576
#define N_ 128
#define F_ 4608

#define WS_MODP   0u
#define WS_MODF   (128u << 10)
#define WS_FLAG   (160u << 10)
#define WS_SD     (164u << 10)
#define WS_U12    (192u << 10)
#define WS_STAT   (176u << 10)
#define WS_XQ     (1u << 20)
#define WS_KR     (4u << 20)
#define WS_X2O    (8u << 20)
#define WS_W1     (12u << 20)
#define WS_WD     (24u << 20)
#define WS_H      (32u << 20)
#define WS_PART   (64u << 20)
#define WS_TMP    (96u << 20)
#define STREAM_B  (41 * 8192)

__device__ __forceinline__ float bf2f(u16 v) { return __uint_as_float(((unsigned)v) << 16); }
__device__ __forceinline__ u16 f2bf(float f) {
    __bf16 b = (__bf16)f;
    return __builtin_bit_cast(u16, b);
}
__device__ __forceinline__ float rbf(float f) { return bf2f(f2bf(f)); }
__device__ __forceinline__ unsigned pkbf(float a, float b) {
    f2 p = {a, b};
    return __builtin_bit_cast(unsigned, __builtin_convertvector(p, bf2));
}

template <int B, class F, int... I>
__device__ __forceinline__ void sf_impl(F&& f, std::integer_sequence<int, I...>) {
    (f(std::integral_constant<int, B + I>{}), ...);
}
template <int I, int N, class F>
__device__ __forceinline__ void static_for(F&& f) {
    if constexpr (N - I > 128) {
        static_for<I, I + 128>(f);
        static_for<I + 128, N>(f);
    } else if constexpr (I < N) sf_impl<I>(f, std::make_integer_sequence<int, N - I>{});
}
template <int N>
__device__ __forceinline__ void wait_vm() {
    asm volatile("s_waitcnt vmcnt(%0)" ::"n"(N > 63 ? 63 : N) : "memory");
}
__device__ __forceinline__ void gload16(u4& r, unsigned voff, const char* base) {
    asm volatile("global_load_dwordx4 %0, %1, %2" : "=v"(r) : "v"(voff), "s"(base));
}
__device__ __forceinline__ void glds16(unsigned lds, unsigned voff, const char* base) {
    asm volatile("s_mov_b32 m0, %0\n\ts_nop 0\n\tglobal_load_lds_dwordx4 %1, %2" ::"s"(lds), "v"(voff), "s"(base) : "memory");
}
typedef __attribute__((address_space(3))) const u4* lds_u4p;
typedef __attribute__((address_space(3))) const f4* lds_f4p;
__device__ __forceinline__ u4 ldsr(unsigned a) { return *(lds_u4p)(size_t)a; }
__device__ __forceinline__ f4 ldsrf(unsigned a) { return *(lds_f4p)(size_t)a; }
__device__ __forceinline__ unsigned lds_addr(char* p) {
    return (unsigned)(size_t)(__attribute__((address_space(3))) char*)p;
}
__device__ __forceinline__ void mfma(f4& c, const u4& a, const u4& b) {
    asm volatile("v_mfma_f32_16x16x32_bf16 %0, %1, %2, %0" : "+v"(c) : "v"(a), "v"(b));
}
__device__ __forceinline__ void mfma0(f4& c, const u4& a, const u4& b) {
    asm volatile("v_mfma_f32_16x16x32_bf16 %0, %1, %2, 0" : "=&v"(c) : "v"(a), "v"(b));
}
template <int N>
__device__ __forceinline__ void wait_vm_reg(u4& r) {
    asm volatile("s_waitcnt vmcnt(%1)" : "+v"(r) : "n"(N > 63 ? 63 : N));
}

#define TSK 1
#define TSDECL(k)
#define TS(i)
#define TSR(i)
#define TSFLUSH(n)
__host__ __device__ inline int w1_col(int c18, int np, int r) { return 288 * np + 96 * (c18 / 6) + 6 * r + c18 % 6; }
__host__ __device__ inline int h_col(int s, int q, int e) {
    int np = s / 9, r9 = s % 9;
    int c18 = r9 < 8 ? 9 * (r9 >> 2) + 2 * (r9 & 3) + e : 9 * e + 8;
    return w1_col(c18, np, 4 * q);
}
__host__ __device__ constexpr int g2_pos(int s) {
    int np = s / 9, r9 = s % 9;
    return r9 == 8 ? 16 + np : (r9 & 2) ? 8 + (r9 & 1) * 4 + np * 2 + (r9 >> 2) : np * 4 + (r9 >> 2) * 2 + (r9 & 1);
}
__host__ __device__ constexpr int g2_inv(int k) {
    return k >= 16 ? (k - 16) * 9 + 8 : k < 8 ? (k >> 2) * 9 + ((k >> 1) & 1) * 4 + (k & 1) : ((k >> 1) & 1) * 9 + (k & 1) * 4 + 2 + ((k - 8) >> 2);
}
__host__ __device__ inline int g2_slot(int p, int ch, int ks) {
    if (ch == 8) return ks < 12 ? ks * 7 : 48 + ks * 3;
    int g = ((ch >> 1) - p - 1) & 3;
    if (g == 3) return 126 + ks * 2 + (ch & 1);
    if (ks < 12) return ks * 7 + 1 + g * 2 + (ch & 1);
    return g == 0 ? 49 + ks * 3 + (ch & 1) : 66 + g * 12 + ks * 2 + (ch & 1);
}
__device__ __forceinline__ float lo2f(unsigned u) { return __builtin_bit_cast(float, u << 16); }
__device__ __forceinline__ float hi2f(unsigned u) { return __builtin_bit_cast(float, u & 0xffff0000u); }
__device__ __forceinline__ void st_sc1(void* p, u4 v) {
    asm volatile("global_store_dwordx4 %0, %1, off sc1\n\ts_nop 1" ::"v"(p), "v"(v) : "memory");
}
template <int CTRL>
__device__ __forceinline__ float dppf(float v) {
    return __builtin_bit_cast(float, __builtin_amdgcn_update_dpp(0, __builtin_bit_cast(int, v), CTRL, 0xf, 0xf, true));
}
__device__ __forceinline__ float row_sum(float v) {
    v += dppf<0xb1>(v); v += dppf<0x4e>(v); v += dppf<0x141>(v); v += dppf<0x140>(v);
    return v;
}
__device__ __forceinline__ float wave_max(float v) {
    v = fmaxf(v, dppf<0xb1>(v)); v = fmaxf(v, dppf<0x4e>(v)); v = fmaxf(v, dppf<0x141>(v)); v = fmaxf(v, dppf<0x140>(v));
    int iv = __builtin_bit_cast(int, v);
    float a = __builtin_bit_cast(float, __builtin_amdgcn_readlane(iv, 0)), b = __builtin_bit_cast(float, __builtin_amdgcn_readlane(iv, 16));
    float c = __builtin_bit_cast(float, __builtin_amdgcn_readlane(iv, 32)), d = __builtin_bit_cast(float, __builtin_amdgcn_readlane(iv, 48));
    return fmaxf(fmaxf(a, b), fmaxf(c, d));
}
typedef __attribute__((ext_vector_type(2))) _Float16 h2;
typedef __attribute__((ext_vector_type(8))) _Float16 h8;
__device__ __forceinline__ f2 cmul(f2 a, f2 b) { return f2{a[0] * b[0] - a[1] * b[1], a[0] * b[1] + a[1] * b[0]}; }
__device__ __forceinline__ f2 cpow(float e2, float rev, float n) {
    float m = __builtin_amdgcn_exp2f(e2 * n), r = __builtin_amdgcn_fractf(rev * n);
    return f2{m * __builtin_amdgcn_cosf(r), m * __builtin_amdgcn_sinf(r)};
}
__device__ __forceinline__ unsigned pkh(f2 v) { return __builtin_bit_cast(unsigned, __builtin_convertvector(v, h2)); }

struct CodeRange {
    const char* pc;
    int len;
};
__device__ __forceinline__ CodeRange code_range() {
    CodeRange c;
    asm volatile("s_getpc_b64 %0\n2:\n\ts_mov_b32 %1, .Lpfe0-2b\n\ts_max_i32 %1, %1, .Lpfe1-2b\n\ts_max_i32 %1, %1, 0" : "=s"(c.pc), "=s"(c.len)::"scc");
    return c;
}
__device__ __forceinline__ unsigned code_pf(const CodeRange& c, int chunk, int lane) {
    unsigned r;
    asm volatile("global_load_dword %0, %1, %2" : "=v"(r) : "v"(min(chunk * 8192 + lane * 128, c.len)), "s"(c.pc));
    return r;
}
#define PFA 1
#define PFB 1
#define PFG 0
#define A_KG 288
#define A_LN 128
#define A_MOD 162
#define A_WD 144
#define A_ZERO 8
#define A_SMEM (4 * 2 * 4352 + 2 * 5184)
struct AArgs {
    const u16 *x, *Are, *Aim, *Bre, *Bim, *Cre, *Cim, *logdt, *c, *wmod, *wd;
    char* ws;
};
__global__ __launch_bounds__(256) void k_a(AArgs A) {
    __shared__ __attribute__((aligned(16))) char smem[A_SMEM];
    const CodeRange cr = code_range();
    int b = blockIdx.x;
    const int tid = threadIdx.x, lane = tid & 63, wid = __builtin_amdgcn_readfirstlane(tid >> 6);
    const int q = lane >> 4, i = lane & 15;
    if (b < A_ZERO) {
        unsigned r[3];
        if (PFA) for (int k = 0; k < 3; k++) r[k] = code_pf(cr, k * 4 + wid, lane);
        if (b == 0)
            for (int k = 0; k < 3; k++) *(u4*)(A.ws + WS_FLAG + (k * 256 + tid) * 16) = u4{0, 0, 0, 0};
        if (PFA) asm volatile("s_waitcnt vmcnt(0)" ::"v"(r[0]), "v"(r[1]), "v"(r[2]));
        return;
    }
    b -= A_ZERO;
    if (b < A_KG) {
        const int d = b * 2 + (wid >> 1), n = d * N_ + (wid & 1) * 64 + lane;
        const float are = bf2f(A.Are[n]), aim = bf2f(A.Aim[n]), bre = bf2f(A.Bre[n]), bim = bf2f(A.Bim[n]);
        const float cre = bf2f(A.Cre[n]), cim = bf2f(A.Cim[n]);
        const float dt = rbf(__builtin_amdgcn_exp2f(bf2f(A.logdt[d]) * 1.44269504f));
        const float ar = dt * are * 1.44269504f, rev = dt * aim * 0.159154943f;
        f2 w = cmul(f2{cre, cim}, f2{dt * bre, dt * bim});
        const float amax = wave_max(ar);
        float wmax = wave_max(fmaxf(fabsf(w[0]), fabsf(w[1])));
        wmax = wmax > 1e-30f ? wmax : 1.f;
        w *= __builtin_amdgcn_rcpf(wmax);
        const float e2 = ar - amax;
        const f2 z1 = cpow(e2, rev, 1.f), z8 = cpow(e2, rev, 8.f), z128 = cpow(e2, rev, 128.f);
        unsigned* buf0 = (unsigned*)(smem + wid * 8704);
        unsigned* buf1 = buf0 + 1088;
        const h8* rd0 = (const h8*)(buf0 + i * 68 + q * 4);
        const h8* rd1 = (const h8*)(buf1 + i * 68 + q * 4);
        {
            f2 v = {1.f, 0.f};
            const f2 z8c = {z8[0], -z8[1]};
            for (int c = 0; c < 16; c++) { buf0[c * 68 + lane] = pkh(v); v = cmul(v, z8c); }
        }
        f2 zp[8];
        zp[1] = z1;
        for (int r = 2; r < 8; r++) zp[r] = cmul(zp[r - 1], z1);
        f2 g = w;
        for (int k = 0; k < 2; k++) {
            buf1[(k * 8) * 68 + lane] = pkh(g);
            for (int r = 1; r < 8; r++) buf1[(k * 8 + r) * 68 + lane] = pkh(cmul(g, zp[r]));
            g = cmul(g, z128);
        }
        h8 vf[4];
        f4 acc[3];
        for (int k = 0; k < 4; k++) vf[k] = rd0[k * 4];
        acc[0] = f4{0.f, 0.f, 0.f, 0.f};
        for (int k = 0; k < 4; k++) acc[0] = __builtin_amdgcn_mfma_f32_16x16x32_f16(rd1[k * 4], vf[k], acc[0], 0, 0, 0);
        for (int k = 0; k < 2; k++) {
            buf0[(k * 4) * 68 + lane] = pkh(g);
            for (int r = 1; r < 4; r++) buf0[(k * 4 + r) * 68 + lane] = pkh(cmul(g, zp[2 * r]));
            g = cmul(g, z128);
        }
        for (int k = 0; k < 4; k++) {
            buf0[(8 + k * 2) * 68 + lane] = pkh(g);
            buf0[(9 + k * 2) * 68 + lane] = pkh(cmul(g, zp[4]));
            g = cmul(g, z128);
        }
        acc[1] = f4{0.f, 0.f, 0.f, 0.f};
        for (int k = 0; k < 4; k++) acc[1] = __builtin_amdgcn_mfma_f32_16x16x32_f16(rd0[k * 4], vf[k], acc[1], 0, 0, 0);
        for (int k = 0; k < 9; k++) {
            buf1[k * 68 + lane] = pkh(g);
            g = cmul(g, z128);
        }
        acc[2] = f4{0.f, 0.f, 0.f, 0.f};
        for (int k = 0; k < 4; k++) acc[2] = __builtin_amdgcn_mfma_f32_16x16x32_f16(rd1[k * 4], vf[k], acc[2], 0, 0, 0);
        float* T = (float*)(smem + 4 * 8704 + (wid >> 1) * 5184);
        for (int r = 0; r < 4; r++) {
            int u0 = 128 * (q >> 1) + 4 * (q & 1) + r + 8 * i;
            T[u0 * 2 + (wid & 1)] = acc[0][r] * (wmax * __builtin_amdgcn_exp2f(amax * (float)u0));
            int u1 = q < 2 ? 256 + 128 * q + 2 * r : 256 * q + 128 * (r >> 1) + 4 * (r & 1);
            int i1 = q < 2 ? 256 + 64 * q + r + 4 * i : 256 + 64 * q + 32 * (r >> 1) + (r & 1) + 2 * i;
            T[i1 * 2 + (wid & 1)] = acc[1][r] * (wmax * __builtin_amdgcn_exp2f(amax * (float)(u1 + 8 * i)));
            int i2 = 64 * q + 16 * r + i;
            if (i2 <= 128) T[(512 + i2) * 2 + (wid & 1)] = acc[2][r] * (wmax * __builtin_amdgcn_exp2f(amax * (float)(1024 + 8 * i2)));
        }
        __syncthreads();
        const f2* T2 = (const f2*)T;
        auto tv = [&](int idx) { f2 t = T2[idx]; return t[0] + t[1]; };
        char* ke = A.ws + WS_KR + d * 3072;
        const int l128 = tid & 127;
        *(unsigned*)(ke + l128 * 4) = pkbf(tv(639 - l128), tv(640 - l128));
        if ((wid & 1) == 0) {
            int i0 = 510 - 2 * lane;
            *(u2*)(ke + 512 + lane * 8) = u2{pkbf(tv(i0), tv(i0 + 1)), pkbf(tv(i0 + 2), 0.f)};
        } else {
            float v[9];
            const int l0 = 511 - 8 * lane;
            for (int j = 0; j < 9; j++) {
                int l = l0 - j;
                int hf = l >> 1;
                int idx = l < 256 ? (l < 0 ? 0 : l) : 128 + hf + (l & hf & 1);
                v[j] = tv(idx);
            }
            if (lane == 63) v[8] = 0.f;
            *(u4*)(ke + 1024 + lane * 16) = u4{pkbf(v[1], v[2]), pkbf(v[3], v[4]), pkbf(v[5], v[6]), pkbf(v[7], v[8])};
            *(u4*)(ke + 2048 + lane * 16) = u4{pkbf(v[0], v[1]), pkbf(v[2], v[3]), pkbf(v[4], v[5]), pkbf(v[6], v[7])};
        }
        return;
    }
    b -= A_KG;
    if (b < A_LN) {
        const int tok = tid >> 4;
        const u2* xr = (const u2*)(A.x + (size_t)(b * 16 + tok) * D_) + i;
        u2 v[9];
        for (int k = 0; k < 9; k++) v[k] = xr[16 * k];
        float s = 0.f;
        for (int k = 0; k < 9; k++) {
            *(u2*)(smem + (i + 16 * k) * 144 + tok * 8) = v[k];
            s += (lo2f(v[k][0]) + hi2f(v[k][0])) + (lo2f(v[k][1]) + hi2f(v[k][1]));
        }
        const float mean = row_sum(s) * (1.f / D_);
        float s2 = 0.f;
        for (int k = 0; k < 9; k++)
            for (int e = 0; e < 2; e++) {
                float d0 = lo2f(v[k][e]) - mean, d1 = hi2f(v[k][e]) - mean;
                s2 += d0 * d0 + d1 * d1;
            }
        const float var = row_sum(s2) * (1.f / D_);
        if (i == 0) *(unsigned*)(A.ws + WS_STAT + (b * 16 + tok) * 4) = pkbf(mean, __builtin_sqrtf(rbf(rbf(var) + 1e-5f)));
        __syncthreads();
        for (int k = 0; k < 5; k++) {
            int g = k * 256 + tid;
            if (g < 1152) st_sc1(A.ws + WS_XQ + (g >> 3) * 16384 + b * 128 + (g & 7) * 16, *(const u4*)(smem + (g >> 3) * 144 + (g & 7) * 16));
        }
        return;
    }
    b -= A_LN;
    if (b < A_MOD) {
        const int cb = b % 54, kp = b / 54, cg = tid & 7, rg = tid >> 3;
        u4 wv[6];
        float sv[6];
        for (int k = 0; k < 6; k++) {
            int row = kp * 192 + k * 32 + rg;
            wv[k] = *(const u4*)(A.wmod + (size_t)row * (6 * D_) + cb * 64 + cg * 8);
            sv[k] = bf2f(A.c[row]);
        }
        float a[8] = {};
        for (int k = 0; k < 6; k++) {
            float s = rbf(sv[k] * __builtin_amdgcn_rcpf(1.f + __builtin_amdgcn_exp2f(-1.44269504f * sv[k])));
            for (int e = 0; e < 4; e++) { a[2 * e] += s * lo2f(wv[k][e]); a[2 * e + 1] += s * hi2f(wv[k][e]); }
        }
        float* red = (float*)smem;
        *(f4*)(red + rg * 68 + cg * 8) = f4{a[0], a[1], a[2], a[3]};
        *(f4*)(red + rg * 68 + cg * 8 + 4) = f4{a[4], a[5], a[6], a[7]};
        __syncthreads();
        const int col = tid >> 2, part = tid & 3;
        float s = 0.f;
        for (int k = 0; k < 8; k++) s += red[(part * 8 + k) * 68 + col];
        s += dppf<0xb1>(s);
        s += dppf<0x4e>(s);
        if (part == 0) ((float*)(A.ws + WS_MODP))[kp * 3456 + cb * 64 + col] = s;
        return;
    }
    b -= A_MOD;
    {
        const int kc = b / 36, kh = (b % 36) / 18, ks = g2_pos(b % 18);
        const u16* r0 = A.wd + (size_t)h_col(b, q, 0) * D_ + wid * 144;
        const u16* r1 = A.wd + (size_t)h_col(b, q, 1) * D_ + wid * 144;
        unsigned v[5][8];
        for (int P = 0; P < 5; P++)
            for (int j = 0; j < 8; j++)
                v[P][j] = *(const unsigned*)((j < 4 ? r0 : r1) + (j & 3) * 6 * D_ + P * 32 + (P < 4 ? 2 * i : (i & 14)));
        char* dst = A.ws + WS_WD + (size_t)(wid * 4 + kc) * STREAM_B + kh * 1024 + lane * 16;
        for (int P = 0; P < 4; P++)
            for (int e = 0; e < 2; e++) {
                u4 o;
                for (int j = 0; j < 4; j++) o[j] = __builtin_amdgcn_perm(v[P][2 * j + 1], v[P][2 * j], e ? 0x07060302u : 0x05040100u);
                st_sc1(dst + g2_slot(kc, 2 * P + e, ks) * 2048, o);
            }
        u4 o;
        const unsigned sel = (i & 1) ? 0x07060302u : 0x05040100u;
        for (int j = 0; j < 4; j++) o[j] = __builtin_amdgcn_perm(v[4][2 * j + 1], v[4][2 * j], sel);
        st_sc1(dst + g2_slot(kc, 8, ks) * 2048, o);
    }
}

#define B_CONV 144
#define B_PREP 96
#define B_PF (PFB ? 8 : 0)
#define B_HP 0
#define B_R  16640
#define B_Y  (B_R + 4 * 9152)
#define B_SMEM (B_Y + 65536)
template <int O, int OB>
__device__ __forceinline__ void cv_rd(u2& al, u2& ah, u4& bf, unsigned aa, unsigned ba) {
    asm volatile("ds_read2_b32 %0, %2 offset0:%3 offset1:%4\n\tds_read2_b32 %1, %2 offset0:%5 offset1:%6"
                 : "=&v"(al), "=&v"(ah) : "v"(aa), "n"(O), "n"(O + 1), "n"(O + 2), "n"(O + 3));
    asm volatile("ds_read_b128 %0, %1 offset:%2" : "=v"(bf) : "v"(ba), "n"(OB));
}
template <int W>
__device__ __forceinline__ void cv_wait(u2& al, u2& ah, u4& bf) {
    asm volatile("s_waitcnt lgkmcnt(%3)" : "+v"(al), "+v"(ah), "+v"(bf) : "n"(W > 15 ? 15 : W));
}
struct BArgs {
    const u16 *dskip, *wg, *wu;
    char* ws;
};
__global__ __launch_bounds__(512, 2) void k_b(BArgs A) {
    extern __shared__ __attribute__((aligned(16))) char smem[];
    const CodeRange cr = code_range();
    int b = blockIdx.x;
    const int tid = threadIdx.x, lane = tid & 63, wid = __builtin_amdgcn_readfirstlane(tid >> 6);
    const int q = lane >> 4, i = lane & 15;
    const float* modp = (const float*)(A.ws + WS_MODP);
    if (b < B_PF) {
        const unsigned r0 = code_pf(cr, wid, lane);
        asm volatile("s_waitcnt vmcnt(0)" ::"v"(r0));
        return;
    }
    b -= B_PF;
    if (b < B_CONV) {
        const int Q = (b >> 4) * 16 + (b & 7) * 2 + ((b >> 3) & 1);
        const unsigned sm = lds_addr(smem);
        const char* ke = A.ws + WS_KR + Q * 4 * 3072;
        glds16(sm + B_R + (wid >> 1) * 9152 + (wid & 1) * 4576 + 3072, lane * 16, ke + (wid >> 1) * 3072 + 1024 + (wid & 1) * 1024);
        const u4* xp = (const u4*)(A.ws + WS_XQ + Q * 16384 + tid * 32);
        const u4 xa = xp[0], xb = xp[1];
        const u4 st = *(const u4*)(A.ws + WS_STAT + tid * 16);
        const unsigned e3 = *(const unsigned*)(ke + (tid >> 7) * 3072 + (tid & 127) * 4);
        const u2 e2 = *(const u2*)(ke + (tid >> 7) * 3072 + 512 + lane * 8);
        float sa[4], c1[4], ga[4], dk[4];
        for (int c = 0; c < 4; c++) {
            const float* m = modp + Q * 4 + c;
            sa[c] = rbf(m[0] + m[3456] + m[6912]);
            c1[c] = rbf(1.f + rbf(m[576] + m[3456 + 576] + m[6912 + 576]));
            ga[c] = rbf(m[1152] + m[3456 + 1152] + m[6912 + 1152]);
            dk[c] = bf2f(A.dskip[Q * 4 + c]);
        }
        if (lane < 28) *(u4*)(smem + B_R + (wid >> 1) * 9152 + (wid & 1) * 4576 + 4096 + lane * 16) = u4{0, 0, 0, 0};
        else if (lane < 32) *(u4*)(smem + B_HP + (wid & 3) * 4160 + (lane - 28) * 16) = u4{0, 0, 0, 0};
        {
            char* r = smem + B_R + (tid >> 7) * 9152;
            const unsigned aa = __builtin_amdgcn_perm(e3, e3, 0x01000100u), bb = __builtin_amdgcn_perm(e3, e3, 0x03020302u);
            const unsigned ba = __builtin_amdgcn_perm(e3, e3, 0x01000302u);
            const bool modd = (tid & 1) == 0;
            *(u4*)(r + (tid & 127) * 16) = u4{bb, modd ? ba : aa, aa, aa};
            *(u4*)(r + 4576 + (tid & 127) * 16) = u4{bb, modd ? bb : ba, aa, aa};
            const unsigned t00 = __builtin_amdgcn_perm(e2[0], e2[0], 0x01000100u), t11 = __builtin_amdgcn_perm(e2[0], e2[0], 0x03020302u);
            const unsigned s0 = (wid & 1) ? 0x05040504u : 0x03020504u, s2 = (wid & 1) ? 0x01000302u : 0x01000100u;
            *(u4*)(r + (wid & 1) * 4576 + 2048 + lane * 16) =
                u4{__builtin_amdgcn_perm(e2[1], e2[0], s0), t11, __builtin_amdgcn_perm(e2[0], e2[0], s2), t00};
        }
        unsigned hd[4][2];
        {
            float hv[4][4];
            for (int t = 0; t < 4; t++) {
                const float m = lo2f(st[t]), rs = __builtin_amdgcn_rcpf(hi2f(st[t]));
                for (int e = 0; e < 2; e++) {
                    const unsigned xw = t < 2 ? xa[2 * t + e] : xb[2 * t - 4 + e];
                    unsigned u = pkbf(lo2f(xw) - m, hi2f(xw) - m);
                    u = pkbf(lo2f(u) * rs, hi2f(u) * rs);
                    u = pkbf(lo2f(u) * c1[2 * e], hi2f(u) * c1[2 * e + 1]);
                    hv[2 * e][t] = lo2f(u) + sa[2 * e];
                    hv[2 * e + 1][t] = hi2f(u) + sa[2 * e + 1];
                }
            }
            for (int c = 0; c < 4; c++) {
                u2 h = {pkbf(hv[c][0], hv[c][1]), pkbf(hv[c][2], hv[c][3])};
                *(u2*)(smem + B_HP + c * 4160 + 64 + tid * 8) = h;
                for (int e = 0; e < 2; e++) hd[c][e] = pkbf(lo2f(h[e]) * dk[c], hi2f(h[e]) * dk[c]);
            }
        }
        wait_vm<0>();
        __syncthreads();
        {
            const int ch = wid >> 1, hf = wid & 1;
            const unsigned ab = sm + B_HP + ch * 4160 + (hf * 64 + 4 * q + i + 1) * 4;
            const unsigned bbase = sm + B_R + ch * 9152 + (i & 1) * 4576 + 448 - 64 * (i >> 1) + 256 * hf + 16 * q;
            f4 acc[8];
            for (int c = 0; c < 8; c++) acc[c] = f4{0.f, 0.f, 0.f, 0.f};
            static_for<0, 4>([&](auto RI) {
                constexpr int rho = decltype(RI)::value;
                u2 al[8], ah[8];
                u4 bf[8];
                static_for<0, 8>([&](auto MI) {
                    constexpr int mu = decltype(MI)::value;
                    constexpr int o = (mu & 1) * 128 + rho * 16;
                    cv_rd<o, (7 - mu) * 512 + rho * 64>(al[mu], ah[mu], bf[mu], ab + (mu >> 1) * 1024, bbase);
                });
                static_for<0, 8>([&](auto CI) {
                    constexpr int n = decltype(CI)::value;
                    constexpr int w = (7 - n) * 3;
                    cv_wait<w>(al[n], ah[n], bf[n]);
                    for (int k = 0; k <= n; k++) {
                        if (n + k < 8) {
                            u4 an = {al[n][0], al[n][1], ah[n][0], ah[n][1]};
                            acc[n + k] = __builtin_amdgcn_mfma_f32_16x16x32_bf16(__builtin_bit_cast(bf8, an), __builtin_bit_cast(bf8, bf[k]), acc[n + k], 0, 0, 0);
                            if (k < n) {
                                u4 ak = {al[k][0], al[k][1], ah[k][0], ah[k][1]};
                                acc[n + k] = __builtin_amdgcn_mfma_f32_16x16x32_bf16(__builtin_bit_cast(bf8, ak), __builtin_bit_cast(bf8, bf[n]), acc[n + k], 0, 0, 0);
                            }
                        }
                    }
                });
            });
            char* yw = smem + B_Y + hf * 32768 + ch * 8192 + (i & 1) * 4096 + (16 * (i >> 1) + 4 * q) * 4;
            for (int c = 0; c < 8; c++) *(f4*)(yw + c * 512) = acc[c];
        }
        __syncthreads();
        float x2[4][4];
        for (int c = 0; c < 4; c++) {
            const char* yr = smem + B_Y + c * 8192 + tid * 8;
            const f2 y0 = *(const f2*)yr + *(const f2*)(yr + 32768), y1 = *(const f2*)(yr + 4096) + *(const f2*)(yr + 32768 + 4096);
            for (int e = 0; e < 2; e++) {
                unsigned u = pkbf(y0[e], y1[e]);
                u = pkbf(lo2f(u) + lo2f(hd[c][e]), hi2f(u) + hi2f(hd[c][e]));
                u = pkbf(lo2f(u) * ga[c], hi2f(u) * ga[c]);
                const unsigned x0 = e ? xb[c >> 1] : xa[c >> 1], x1 = e ? xb[2 + (c >> 1)] : xa[2 + (c >> 1)];
                x2[2 * e][c] = ((c & 1) ? hi2f(x0) : lo2f(x0)) + lo2f(u);
                x2[2 * e + 1][c] = ((c & 1) ? hi2f(x1) : lo2f(x1)) + hi2f(u);
            }
        }
        char* xo = A.ws + WS_X2O + (Q >> 1) * 32768 + tid * 64 + (Q & 1) * 8;
        for (int t = 0; t < 4; t++) *(u2*)(xo + t * 16) = u2{pkbf(x2[t][0], x2[t][1]), pkbf(x2[t][2], x2[t][3])};
        return;
    }
    b -= B_CONV;
    {
        const int up = b / 48, np = (b % 48) / 3, un = b % 3;
        const char* src = (const char*)((up ? A.wu : A.wg) + (size_t)(wid * 32 + q * 8) * F_ + 288 * np + 96 * un + 6 * i);
        typedef __attribute__((ext_vector_type(3))) unsigned u3;
        typedef u3 __attribute__((aligned(4))) u3a;
        u3 v[3][8];
        for (int k = 0; k < 2; k++)
            for (int j = 0; j < 8; j++) v[k][j] = *(const u3a*)(src + (size_t)(k * 256 + j) * (F_ * 2));
        if (wid < 2)
            for (int j = 0; j < 8; j++) v[2][j] = *(const u3a*)(src + (size_t)(512 + j) * (F_ * 2));
        float* tc = (float*)smem;
        float* tsb = tc + 576;
        float* red = tc + 1152;
        if (tid < 288) {
            const f2* m = (const f2*)(modp + 3 * D_) + tid;
            f2 sb = m[0] + m[1728] + m[3456], cb = m[288] + m[1728 + 288] + m[3456 + 288];
            *(f2*)(tc + 2 * tid) = f2{rbf(1.f + rbf(cb[0])), rbf(1.f + rbf(cb[1]))};
            *(f2*)(tsb + 2 * tid) = f2{rbf(sb[0]), rbf(sb[1])};
            if (b == 0) {
                f2 gb = m[576] + m[1728 + 576] + m[3456 + 576];
                *(f2*)(A.ws + WS_MODF + (5 * D_ + 2 * tid) * 4) = f2{rbf(gb[0]), rbf(gb[1])};
            }
        }
        __syncthreads();
        float s1[6] = {}, s2[6] = {};
        auto pass1 = [&](int k) {
            const int k0 = (wid + 8 * k) * 32 + q * 8;
            for (int j = 0; j < 8; j++) {
                const float c = tc[k0 + j], s = tsb[k0 + j];
                for (int e = 0; e < 3; e++) {
                    const float wl = lo2f(v[k][j][e]), wh = hi2f(v[k][j][e]);
                    s1[2 * e] += wl * c; s2[2 * e] += wl * s;
                    s1[2 * e + 1] += wh * c; s2[2 * e + 1] += wh * s;
                }
            }
        };
        pass1(0); pass1(1);
        if (wid < 2) pass1(2);
        {
            float t[6];
            for (int e = 0; e < 3; e++) {
                asm volatile("s_nop 1\n\tv_permlane16_swap_b32 %0, %1\n\tv_permlane16_swap_b32 %2, %3" : "+v"(s1[e]), "+v"(s1[3 + e]), "+v"(s2[e]), "+v"(s2[3 + e]));
                t[e] = s1[e] + s1[3 + e];
                t[3 + e] = s2[e] + s2[3 + e];
            }
            for (int e = 0; e < 3; e++) {
                asm volatile("s_nop 1\n\tv_permlane32_swap_b32 %0, %1" : "+v"(t[e]), "+v"(t[3 + e]));
                red[((3 * q + e) * 16 + i) * 8 + wid] = t[e] + t[3 + e];
            }
        }
        __syncthreads();
        float mean[6];
        for (int c = 0; c < 6; c++) {
            const f4 a = *(const f4*)(red + (c * 16 + i) * 8), bq = *(const f4*)(red + (c * 16 + i) * 8 + 4);
            const f4 s = a + bq;
            mean[c] = ((s[0] + s[1]) + (s[2] + s[3])) * (1.f / D_);
        }
        if (tid < 96) {
            const f4 a = *(const f4*)(red + (96 + tid) * 8), bq = *(const f4*)(red + (96 + tid) * 8 + 4);
            const f4 s = a + bq;
            const int c18 = un * 6 + (tid >> 4);
            *(float*)(A.ws + WS_U12 + (2 * np + c18 / 9) * 2048 + ((c18 % 9) * 32 + up * 16 + (tid & 15)) * 4) = (s[0] + s[1]) + (s[2] + s[3]);
        }
        auto pass2 = [&](int k) {
            const int ks = wid + 8 * k, k0 = ks * 32 + q * 8;
            float c[8];
            for (int j = 0; j < 8; j++) { c[j] = tc[k0 + j]; asm volatile("" : "+v"(v[k][j])); }
            for (int cc = 0; cc < 6; cc++) {
                u4 o;
                for (int j = 0; j < 4; j++) {
                    const unsigned w0 = v[k][2 * j][cc >> 1], w1 = v[k][2 * j + 1][cc >> 1];
                    o[j] = pkbf(((cc & 1) ? hi2f(w0) : lo2f(w0)) * c[2 * j] - mean[cc], ((cc & 1) ? hi2f(w1) : lo2f(w1)) * c[2 * j + 1] - mean[cc]);
                }
                const int c18 = un * 6 + cc, nt = 2 * np + c18 / 9, ch = c18 % 9;
                const int fi = ch < 4 ? ks * 8 + ch * 2 + up : ch < 8 ? (ch >> 1) * 72 + ks * 4 + (ch & 1) * 2 + up : 288 + ks * 2 + up;
                st_sc1(A.ws + WS_W1 + (size_t)nt * STREAM_B + fi * 1024 + lane * 16, o);
            }
        };
        pass2(0); pass2(1);
        if (wid < 2) pass2(2);
    }
}

#if defined(__HIP_DEVICE_COMPILE__)
#define NOPK __attribute__((target("no-packed-fp32-ops")))
#else
#define NOPK
#endif
#define G_WT 32
#define G_WU 32
#define G_WU2 56
#define G_HS 16
#define G_TZ0 44
#define G_TZS 24
#define G_LK 3
#define G_HE 6
#define G_TSL -16
#define G_TST 12
#define G_UE 23
#define G_PACE 5
#define G_URG 36
#define G_TL 150
#define G_PRIO 1
#define G_LEADHI 1
namespace gf {
constexpr int NS1 = 324, NS = 486, LAG = 32, NU = 82, RING = 19, WT = G_WT, WU = G_WU, TSD = 110;
constexpr int NSYNC = 26, LK = G_LK;
constexpr int PP[2][NSYNC + 1] = {
    {0, 8, 16, 32, 48, 64, 80, 104, 128, 176, 192, 208, 232, 256, 280, 304, 322, 340, 356, 372, 392, 412, 428, 440, 452, 468, 486},
    {-32, -24, -16, 0, 16, 32, 48, 72, 96, 144, 168, 192, 216, 240, 264, 288, 308, 324, 348, 372, 392, 412, 428, 440, 452, 468, 486}};
constexpr int JFLAG[4] = {12, 14, 16, 18};
constexpr int TPL[2][4] = {{248, 300, 342, 374}, {234, 292, 330, 374}};
constexpr int TPC[2][4] = {{260, 310, 352, 384}, {246, 302, 340, 384}};
constexpr int TEP[4] = {143, 215, 287, 323};
constexpr int THS[9] = {143, 143 + G_HS, 143 + 2 * G_HS, 143 + 3 * G_HS, 215, 215 + G_HS, 287, 287, 323};
constexpr int TZ0 = G_TZ0, TZS = G_TZS, TSTAT = G_TST;
constexpr int isflag(int j) { return j == JFLAG[0] ? 0 : j == JFLAG[1] ? 1 : j == JFLAG[2] ? 2 : j == JFLAG[3] ? 3 : -1; }
constexpr int TX[3] = {NS1 + 104, NS1 + 116, NS1 + 128}, TMISC = NS1 + 134, TL = NS1 + G_TL;
constexpr int hneed(int ks) { return NS1 + (ks < 12 ? ks * 7 : 48 + ks * 3); }
constexpr int ufirst(int u) { return u < 41 ? 8 * u : NS1 + 4 * (u - 41); }
constexpr int ulast(int u) { return u < 40 ? 8 * u + 7 : u == 40 ? NS1 - 1 : NS1 + 4 * (u - 41) + 3; }
constexpr int page(int u) { int r = u < 80 ? (u + 11) % RING : u - 80; return r < 15 ? r : r + 1; }
constexpr int prio(int r, int t) {
    int j = 0;
    while (PP[r][j + 1] <= t) j++;
    int e = -1;
    for (int g = 0; g < 4; g++)
        if (PP[0][j] <= TEP[g] && TEP[g] < PP[0][j + 1]) e = TEP[g];
    if (e >= 0) return r ? (t == PP[r][j] ? 2 : -1) : t == PP[r][j] ? 3 : t == e + 1 ? 0 : -1;
    int n = PP[r][j + 1] - PP[r][j], a = (t - PP[r][j]) * 4 / n;
    return t == PP[r][j] || a != (t - 1 - PP[r][j]) * 4 / n ? 3 - a : -1;
}
constexpr int NT = NS + LAG;
struct Sched {
    int nop;
    int op[200];
    int lo[NT + 1];
    int sync_at[NT];
    int sync_wait[NSYNC];
    int tok_wait[18], h_wait[18], poll_wait[4];
    int stat_wait, sd_wait, minlead;
    bool ok;
};
constexpr Sched make(int r) {
    Sched s{};
    s.ok = true;
    s.minlead = 1000;
    const int* P = PP[r];
    const int t0 = P[0];
    int est[320] = {}, need[320] = {}, pos[320] = {}, jv[NU] = {};
    bool live[320] = {};
    for (int u = 0; u < NU; u++) {
        int j = 0;
        while (ufirst(u) > PP[0][j + 1] + LK - 1) j++;
        jv[u] = j;
        need[u] = P[j];
        est[u] = u < G_UE ? P[j] - WU : u < 41 ? P[j] - G_WU2 : 0;
        if (u >= RING) {
            int jf = 0;
            while (PP[1][jf] <= ulast(u < 80 ? u - RING : u - 15)) jf++;
            if (jf >= j) s.ok = false;
            if (est[u] < P[jf]) est[u] = P[jf];
        }
        live[u] = true;
    }
    for (int k = 0; k < 36; k++) {
        need[100 + k] = (k >> 1) * 8;
        est[100 + k] = need[100 + k] - WT + (k & 1) * 4;
        need[200 + k] = hneed(k >> 1);
        int e = k < G_HE ? 0 : 290 + ((k - G_HE) & ~1), c = TPC[r][k < 16 ? 0 : k < 24 ? 1 : k < 32 ? 2 : 3];
        est[200 + k] = e > c ? e : c;
        live[100 + k] = live[200 + k] = true;
    }
    if (r == 1)
        for (int k = 300; k < 304; k++) { need[k] = TSTAT; est[k] = G_TSL; live[k] = true; }
    int n = 0, last = -1000, j = 0, pos_sd = 0, pos_st[4] = {}, pos_fl = 0;
    for (int t = t0 - 1; t < NS; t++) {
        if (t >= t0) {
            s.lo[t - t0] = s.nop;
            s.sync_at[t - t0] = -1;
            if (P[j] == t) {
                s.sync_at[t - t0] = j;
                int m = -1;
                for (int u = 0; u < NU; u++)
                    if (jv[u] <= j) { if (live[u]) s.ok = false; if (pos[u] > m) m = pos[u]; }
                if (r == 1 && j >= 6 && pos[300] > m) m = pos[300];
                int g = isflag(j);
                if (g >= 0 && pos_st[g] > m) m = pos_st[g];
                if (g >= 0 && t <= THS[g == 0 ? 3 : g == 3 ? 8 : 2 * g + 3]) s.ok = false;
                s.sync_wait[j] = n - 1 - m;
                j++;
            }
            for (int g = 0; g < 4; g++)
                if (t == TPC[r][g]) s.poll_wait[g] = n - 1 - pos_fl;
        }
        while (true) {
            int b = -1;
            for (int o = 0; o < 304; o++)
                if (live[o] && est[o] <= t && (b < 0 || need[o] < need[b])) b = o;
            if (b < 0) break;
            if (t >= 0 && need[b] - t > G_URG && 2 * t - last < G_PACE) break;
            live[b] = false;
            pos[b] = n++;
            s.op[s.nop++] = b;
            last = 2 * t - last < G_PACE + 2 && 2 * t - last >= G_PACE ? last + G_PACE : 2 * t;
            if (t >= 40 && need[b] - t < s.minlead) s.minlead = need[b] - t;
            if (t >= 0) break;
        }
        if (r == 1 && t == TSTAT) { if (live[303]) s.ok = false; s.stat_wait = n - 1 - pos[303]; n++; }
        if (t >= 0 && t < 144 && t % 8 == 0) {
            int a = 100 + t / 4;
            if (live[a] || live[a + 1]) s.ok = false;
            s.tok_wait[t / 8] = n - 1 - (pos[a] > pos[a + 1] ? pos[a] : pos[a + 1]);
        }
        for (int ks = 0; ks < 18; ks++)
            if (t == hneed(ks)) {
                int a = 200 + ks * 2;
                if (live[a] || live[a + 1]) s.ok = false;
                s.h_wait[ks] = n - 1 - (pos[a] > pos[a + 1] ? pos[a] : pos[a + 1]);
            }
        if (t == TSD) { n += 2; pos_sd = n - 1; }
        if (t == 143) s.sd_wait = n - 1 - pos_sd;
        for (int i = 0; i < 9; i++)
            if (t == THS[i]) pos_st[i < 4 ? 0 : i / 2 - 1] = n++;
        if (t >= TZ0 && t < TZ0 + 4 * TZS && (t - TZ0) % TZS == 0) n++;
        for (int g = 0; g < 4; g++)
            if (t == TPL[r][g]) pos_fl = n++;
        if (t == TX[0] + 2 || t == TX[0] + 4 || t == TX[1] + 3 || t == TX[2] + 3 || (t >= TMISC && t < TMISC + 5)) n++;
    }
    s.lo[NS - t0] = s.nop;
    if (j != NSYNC || TL < P[NSYNC - 1] || TZ0 + 3 * TZS >= 143) s.ok = false;
    return s;
}
template <int R> struct SS { static constexpr Sched s = make(R); };
static_assert(SS<0>::s.ok && SS<1>::s.ok, "bad schedule");
constexpr int TAB_OFF = 15 * 8192, XA_OFF = 15 * 8192, XB_OFF = 18 * 8192, SMEM = 20 * 8192;

template <int N>
__device__ __forceinline__ void wait_vm_reg2(u4& a, u4& b) {
    asm volatile("s_waitcnt vmcnt(%2)" : "+v"(a), "+v"(b) : "n"(N > 63 ? 63 : N));
}
__device__ __forceinline__ float half_sum(float v) {
    v = row_sum(v);
    float w = v;
    asm volatile("s_nop 1\n\tv_permlane16_swap_b32 %0, %1\n\ts_nop 1" : "+v"(v), "+v"(w));
    return v + w;
}
typedef __attribute__((address_space(3))) f4* lds_f4w;
__device__ __forceinline__ void gload_sc1(u4& r, unsigned voff, const char* base) {
    asm volatile("global_load_dwordx4 %0, %1, %2 sc1" : "=v"(r) : "v"(voff), "s"(base));
}
__device__ __forceinline__ void st16(u4 o, unsigned voff, char* base) {
    asm volatile("global_store_dwordx4 %0, %1, %2 sc1\n\ts_nop 1" ::"v"(voff), "v"(o), "s"(base) : "memory");
}
template <int HI>
__device__ __forceinline__ void st4(unsigned o, unsigned voff, char* base) {
    asm volatile("s_mov_b32 exec_lo, 1\n\ts_mov_b32 exec_hi, %3\n\tglobal_store_dword %0, %1, %2 sc1\n\ts_mov_b64 exec, -1" ::"v"(voff), "v"(o), "s"(base), "n"(HI) : "memory");
}
__device__ __forceinline__ void ld4(unsigned& o, unsigned voff, const char* base) {
    asm volatile("global_load_dword %0, %1, %2 sc1" : "=v"(o) : "v"(voff), "s"(base));
}
__device__ __forceinline__ void pub(u4 o, unsigned voff, char* base) {
    o[0] = o[0] > 1u ? o[0] : 1u;
    o[2] = o[2] > 1u ? o[2] : 1u;
    st16(o, voff, base);
}
template <int O>
__device__ __forceinline__ void st16o(u4 o, unsigned voff, char* base) {
    asm volatile("global_store_dwordx4 %0, %1, %2 offset:%3 sc1\n\ts_nop 1" ::"v"(voff), "v"(o), "s"(base), "n"(O) : "memory");
}
__device__ __forceinline__ void swap2(u4& a, u4& b) {
    unsigned a2 = a[2], a3 = a[3], b0 = b[0], b1 = b[1];
    asm volatile("v_swap_b32 %0, %2\n\tv_swap_b32 %1, %3\n\ts_nop 1" : "+v"(a2), "+v"(a3), "+v"(b0), "+v"(b1));
    a[2] = a2; a[3] = a3; b[0] = b0; b[1] = b1;
}
template <int N>
__device__ __forceinline__ void poll(unsigned& fl, unsigned voff, const char* base, unsigned long long mask) {
    asm volatile(
        "s_waitcnt vmcnt(%4)\n"
        "1:\n\t"
        "v_cmp_eq_u32 vcc, 0, %0\n\t"
        "s_nop 1\n\t"
        "s_and_b64 vcc, vcc, %3\n\t"
        "s_cbranch_scc0 2f\n\t"
        "s_sleep 1\n\t"
        "global_load_dword %0, %1, %2 sc1\n\t"
        "s_waitcnt vmcnt(0)\n\t"
        "s_branch 1b\n"
        "2:"
        : "+v"(fl)
        : "v"(voff), "s"(base), "s"(mask), "n"(N > 63 ? 63 : N)
        : "vcc", "scc", "memory");
}

template <int R>
__device__ __forceinline__ void body(unsigned sm, char* ws, char* out, int wid, int lane, unsigned pf) {
    using S = SS<R>;
    constexpr int T0 = PP[R][0];
    TSDECL(1);
    TSR(0); TS(1);
    const int b = blockIdx.x, xcd = b & 7, idx = b >> 3;
    const int mt = (xcd & 1) * 4 + (idx & 3), nt = (xcd >> 1) * 8 + (idx >> 2);
    const int p = xcd >> 1, mt2 = (xcd & 1) * 8 + (idx & 7), nt2 = idx >> 3;
    const int tk = (wid & 3) * 2 + R;
    unsigned lb0 = sm + lane * 16;
    asm volatile("" : "+v"(lb0));
    char* wb = ws - sm;
    const char* xsrc = ws + WS_X2O;
    const unsigned voff = ((lane >> 4) * 2048 + mt * 256 + wid * 32 + (lane & 15)) * 16;
    const char* wsrc = wb + WS_W1 + (size_t)nt * STREAM_B + wid * 1024;
    const char* wsrc2 = wb + WS_WD + (size_t)(nt2 * 4 + p) * STREAM_B + wid * 1024;
    char* hdst = wb + WS_H + ((size_t)((mt * 16 + wid * 2) * 144 + (nt >> 1) * 9) * 64) * 16 + (nt & 1) * 4096;
    char* hdst1 = hdst + 147456;
    const char* h0 = wb + WS_H + (size_t)((mt2 * 8 + tk) * 144 + p * 36 + R * 18) * 1024;
    const char* h1 = h0 + (R ? -147456 : 147456);
    char* sdp = ws + WS_SD + mt * 1024;
    char* part = wb + WS_PART + (size_t)(mt2 * 4 + nt2) * (128 * 1024);
    const char* flp = ws + WS_FLAG + (mt2 >> 1) * 512;
    const unsigned wdst = sm + wid * 1024;
    u4 tok[36], hk[36], st[3];
    auto issue1 = [&](auto I) {
        constexpr int op = S::s.op[decltype(I)::value];
        if constexpr (op < 41) glds16(wdst + page(op) * 8192, lb0, wsrc + op * 8192);
        else if constexpr (op < 100) glds16(wdst + page(op) * 8192, lb0, wsrc2 + (op - 41) * 8192);
        else if constexpr (op < 200) gload16(tok[op - 100], voff, xsrc + ((op - 100) >> 1) * 131072 + ((op - 100) & 1) * 256);
        else if constexpr (op < 300) gload_sc1(hk[op - 200], lb0, ((op & 1) ? h1 : h0) + g2_inv((op - 200) >> 1) * 1024);
        else if constexpr (op == 300) glds16(sm + TAB_OFF + (wid & 1) * 1024, lb0, wb + WS_U12 + nt * 2048 + (wid & 1) * 1024);
        else gload16(st[op - 301], (lane & (op == 303 ? 7 : 31)) * 32768 + (lane >> 5) * 16,
                     xsrc + (mt * 256 + nt * 8 + (wid & 3) * 2) * 16 + (op - 301) * 32 * 32768);
    };
    static_for<0, S::s.lo[0]>(issue1);
    TS(2);
    unsigned lbc = 0;
    auto frag = [&](auto FI) -> u4 {
        constexpr int f = decltype(FI)::value;
        constexpr auto foff = [](int f) { return f < NS1 ? page(f / 8) * 8192 + (f % 8) * 1024 : page(41 + (f - NS1) / 4) * 8192 + ((f - NS1) % 4) * 2048 + R * 1024; };
        constexpr int o = foff(f);
        if constexpr (f == 0 || (foff(f - 1) >> 16) != (o >> 16)) {
            if constexpr (o >= 65536) { lbc = lb0 + (o & ~65535); asm volatile("" : "+v"(lbc)); }
        }
        return o < 65536 ? ldsr(lb0 + o) : ldsr(lbc + (o & 65535));
    };
    f4 acc[8][2], aco[2][2], xv[7], gb0, gb1;
    u4 fr[LK + 1], x2v, hs[4], pl[4];
    float rs[2] = {0.f, 0.f};
    unsigned sdv[2], fl, x2c, gbc, xw = 0, xr = 0, off8 = 0;
    int trow = 0;
    const char* gbp = ws + WS_MODF + 5 * D_ * 4;
    static_for<T0, NS>([&](auto TI) {
        constexpr int t = decltype(TI)::value;
        if constexpr (S::s.sync_at[t - T0] >= 0) {
            constexpr int j = S::s.sync_at[t - T0];
            __builtin_amdgcn_sched_barrier(0);
            if constexpr (t == TX[0] || t == TX[1] || t == TX[2]) asm volatile("s_waitcnt lgkmcnt(0)" ::: "memory");
            wait_vm<S::s.sync_wait[j]>();
            __builtin_amdgcn_s_barrier();
            asm volatile("" ::: "memory");
            TS(3 + j);
            if constexpr (j == 1) asm volatile("" ::"v"(pf));
            if constexpr (R == 0 && isflag(j) >= 0)
                if ((wid & 3) == 0) st4<0>(1u, 0u, ws + WS_FLAG + mt * 512 + nt * 4 + isflag(j) * 128);
            __builtin_amdgcn_sched_barrier(0);
        }
        if constexpr (G_PRIO && t >= 0 && prio(R, t) >= 0) __builtin_amdgcn_s_setprio(prio(R, t));
        if constexpr (t == TPC[R][0]) poll<S::s.poll_wait[0]>(fl, (lb0 - sm) >> 2, flp, 15ull << (p * 8 + R * 4));
        if constexpr (t == TPC[R][1]) poll<S::s.poll_wait[1]>(fl, (lb0 - sm) >> 2, flp, (15ull << (32 + p * 8 + R * 4)) | 0xffffffffull);
        if constexpr (t == TPC[R][2]) poll<S::s.poll_wait[2]>(fl, (lb0 - sm) >> 2, flp + 256, 15ull << (p * 8 + R * 4));
        if constexpr (t == TPC[R][3]) poll<S::s.poll_wait[3]>(fl, (lb0 - sm) >> 2, flp + 256, 15ull << (32 + p * 8 + R * 4));
        static_for<S::s.lo[t - T0], S::s.lo[t - T0 + 1]>(issue1);
        if constexpr (R == 1 && t == TSTAT) {
            asm volatile("s_waitcnt vmcnt(%3)" : "+v"(st[0]), "+v"(st[1]), "+v"(st[2]) : "n"(S::s.stat_wait));
            const bool x = (lane & 31) < 8;
            float v[24];
            for (int i = 0; i < 12; i++) {
                unsigned a = st[i >> 2][i & 3];
                if (i >= 8) a = x ? a : 0u;
                v[2 * i] = lo2f(a);
                v[2 * i + 1] = hi2f(a);
            }
            float a0 = 0.f, a1 = 0.f;
            for (int i = 0; i < 12; i++) { a0 += v[i]; a1 += v[12 + i]; }
            const float mean = half_sum(a0 + a1) * (1.f / D_);
            float q0 = 0.f, q1 = 0.f, q2 = 0.f;
            for (int i = 0; i < 8; i++) {
                float d0 = v[i] - mean, d1 = v[8 + i] - mean, d2 = v[16 + i] - mean;
                q0 += d0 * d0; q1 += d1 * d1; q2 += d2 * d2;
            }
            const float var = half_sum(q0 + q1 + (x ? q2 : 0.f)) * (1.f / D_);
            st4<1>(((unsigned)f2bf(__builtin_sqrtf(rbf(rbf(var) + 1e-5f))) << 16) | 1u, (lane >> 5) * 4, sdp + (nt * 8 + (wid & 3) * 2) * 4);
            __builtin_amdgcn_sched_barrier(0);
        }
        if constexpr (t >= 0) {
            if constexpr (t == 0) static_for<0, LK>([&](auto FI) { fr[decltype(FI)::value] = frag(FI); });
            constexpr int t2 = t - NS1;
            constexpr int ks = t < 144 ? t / 8 : t < 288 ? (t % 72) / 4 : t < NS1 ? (t - 288) / 2 : t2 < 84 ? t2 / 7 : t2 < 102 ? (t2 - 48) / 3 : t2 < 126 ? 12 + (t2 - 102) % 12 / 2 : (t2 - 126) / 2;
            constexpr int a = t < 144 ? t % 8 : t < 288 ? t % 4 : (t < NS1 || t2 >= 126) ? t % 2 : t2 < 84 ? t2 % 7 : t2 < 102 ? t2 % 3 : (t2 < 114 ? 3 : 5) + t % 2;
            if constexpr (t < 144 && a == 0) wait_vm_reg2<S::s.tok_wait[ks]>(tok[2 * ks], tok[2 * ks + 1]);
            if constexpr (t == hneed(ks)) {
                wait_vm_reg2<S::s.h_wait[ks]>(hk[2 * ks], hk[2 * ks + 1]);
                if constexpr (ks >= 16) swap2(hk[2 * ks + R], hk[2 * ks + 1 - R]);
            }
            if constexpr (t + LK < NS) fr[(t + LK) % (LK + 1)] = frag(std::integral_constant<int, t + LK>{});
            for (int sub = 0; sub < 2; sub++) {
                f4& c = t2 >= 126 ? aco[a & 1][sub] : acc[a][sub];
                const u4& bo = t2 >= 0 ? hk[2 * ks + sub] : tok[2 * ks + sub];
                if constexpr (ks == 0) mfma0(c, fr[t % (LK + 1)], bo);
                else mfma(c, fr[t % (LK + 1)], bo);
                __builtin_amdgcn_sched_barrier(0);
            }
            if constexpr (t == TSD) {
                unsigned o = (lb0 - sm) >> 2 & 0x3c;
                asm volatile("global_load_dword %0, %2, %3 sc1\n\tglobal_load_dword %1, %2, %3 offset:64 sc1"
                             : "=&v"(sdv[0]), "=&v"(sdv[1]) : "v"(o), "s"(sdp + wid * 128));
            }
            if constexpr (t == 143) {
                unsigned o = (lb0 - sm) >> 2 & 0x3c, tmp;
                asm volatile(
                    "s_waitcnt vmcnt(%5)\n"
                    "1:\n\t"
                    "v_and_b32 %2, %0, %1\n\t"
                    "v_and_b32 %2, 1, %2\n\t"
                    "v_cmp_eq_u32 vcc, 0, %2\n\t"
                    "s_cbranch_vccz 2f\n\t"
                    "s_sleep 2\n\t"
                    "global_load_dword %0, %3, %4 sc1\n\t"
                    "global_load_dword %1, %3, %4 offset:64 sc1\n\t"
                    "s_waitcnt vmcnt(0)\n\t"
                    "s_branch 1b\n"
                    "2:"
                    : "+v"(sdv[0]), "+v"(sdv[1]), "=&v"(tmp)
                    : "v"(o), "s"(sdp + wid * 128), "n"(S::s.sd_wait)
                    : "vcc");
                for (int sub = 0; sub < 2; sub++) {
                    unsigned bb = sdv[sub] & 0xffff0000u;
                    rs[sub] = __builtin_amdgcn_rcpf(__builtin_bit_cast(float, bb));
                }
            }
            if constexpr (t == TEP[0] || t == TEP[1] || t == TEP[2] || t == TEP[3]) {
                constexpr int g = t / 72 - 1;
                __builtin_amdgcn_sched_barrier(0);
                TS(29 + 2 * g);
                asm volatile("s_nop 7");
                constexpr int NC = g == 0 ? 4 : g < 3 ? 2 : 1, C0 = g == 0 ? 0 : 2 * g + 2;
                unsigned tb = sm + TAB_OFF + (((lb0 - sm) >> 8) << 4);
                asm volatile("" : "+v"(tb));
                f4 ta = ldsrf(tb + C0 * 128), tc = ldsrf(tb + C0 * 128 + 64);
                static_for<0, NC>([&](auto CI) {
                    constexpr int c = decltype(CI)::value;
                    f4 ua = ta, uc = tc;
                    if constexpr (c + 1 < NC) {
                        ta = ldsrf(tb + (C0 + c + 1) * 128);
                        tc = ldsrf(tb + (C0 + c + 1) * 128 + 64);
                    }
                    for (int sub = 0; sub < 2; sub++) {
                        f4 pr = (acc[2 * c][sub] * rs[sub] + ua) * (acc[2 * c + 1][sub] * rs[sub] + uc);
                        u4& o = hs[g < 3 ? (c & 2) + sub : 0];
                        const int h = g < 3 ? (c & 1) * 2 : sub * 2;
                        o[h] = pkbf(pr[0], pr[1]);
                        o[h + 1] = pkbf(pr[2], pr[3]);
                    }
                    __builtin_amdgcn_sched_barrier(0);
                });
                TS(30 + 2 * g);
            }
            static_for<0, 8>([&](auto II) {
                constexpr int i = decltype(II)::value;
                if constexpr (t == THS[i]) st16o<(i >> 1) * 1024>(hs[i < 4 ? i : i & 1], lb0, (i & 1) ? hdst1 : hdst);
            });
            if constexpr (t == THS[8]) st16(hs[0], lb0, hdst + 8192 + (nt & 1) * (147456 - 4096));
            if constexpr (t >= TZ0 && t < TZ0 + 4 * TZS && (t - TZ0) % TZS == 0)
                st16(u4{0, 0, 0, 0}, lb0, wb + WS_PART + (size_t)(mt * 32 + nt) * 32768 + ((t - TZ0) / TZS * 8 + wid) * 1024);
            if constexpr (t == TPL[R][0] || t == TPL[R][1]) ld4(fl, (lb0 - sm) >> 2, flp);
            if constexpr (t == TPL[R][2] || t == TPL[R][3]) ld4(fl, (lb0 - sm) >> 2, flp + 256);
            if constexpr (t == TX[0] - 3) {
                xw = lb0 + XA_OFF + wid * 1024;
                xr = lb0 + XA_OFF + (wid ^ 4) * 1024;
            }
            static_for<0, 7>([&](auto CI) {
                constexpr int c = decltype(CI)::value, g = c < 3 ? 0 : (c - 1) >> 1, i = c < 3 ? c : (c - 1) & 1;
                constexpr int o = (g == 1 ? XB_OFF - XA_OFF : 0) + i * 8192;
                if constexpr (t == TX[g] - (g ? 2 : 3) + i) *(lds_f4w)(size_t)(xw + o) = acc[c][1];
                if constexpr (t == TX[g] + i) xv[c] = ldsrf(xr + o);
                if constexpr (c == 0 && t == TX[0] + 2) pub(__builtin_bit_cast(u4, acc[0][0] + xv[0]), lb0, part + 96 * 1024 + (p * 8 + tk) * 1024);
                if constexpr ((c & 1) && t == TX[g] + (g ? 3 : 4)) {
                    const f4 va = acc[c][0] + xv[c], vb = acc[c + 1][0] + xv[c + 1];
                    pub(u4{pkbf(va[0], vb[0]), pkbf(va[1], vb[1]), pkbf(va[2], vb[2]), pkbf(va[3], vb[3])}, lb0,
                        part + ((((p + 1 + g) & 3) * 3 + g) * 8 + tk) * 1024);
                }
            });
            if constexpr (t == TMISC) {
                const int q = lane >> 4, tl = lane & 15;
                trow = (mt2 * 8 + tk) * 16 + tl;
                off8 = sm + 96 * 1024 + (q * 8 + tk) * 1024 + (p * 16 + tl) * 16;
                gload16(x2v, ((nt2 * 18 + p * 4 + q) * 2048 + trow) * 16, xsrc);
            }
            if constexpr (t == TMISC + 1)
                asm volatile("global_load_ushort %0, %1, %2" : "=v"(x2c) : "v"(((nt2 * 18 + 16 + (p >> 1)) * 2048 + trow) * 16 + ((p & 1) * 4 + (lane >> 4)) * 2), "s"(xsrc));
            if constexpr (t == TMISC + 2) asm volatile("global_load_dwordx4 %0, %1, %2" : "=v"(gb0) : "v"((lane >> 4) * 32), "s"(gbp + (nt2 * 144 + p * 32) * 4));
            if constexpr (t == TMISC + 3) asm volatile("global_load_dwordx4 %0, %1, %2 offset:16" : "=v"(gb1) : "v"((lane >> 4) * 32), "s"(gbp + (nt2 * 144 + p * 32) * 4));
            if constexpr (t == TMISC + 4) asm volatile("global_load_dword %0, %1, %2" : "=v"(gbc) : "v"((lane >> 4) * 4), "s"(gbp + (nt2 * 144 + 128 + p * 4) * 4));
            if constexpr (t == TL) gload_sc1(pl[3], off8, part);
            if constexpr (t > TL && t < TL + 4) gload_sc1(pl[t - TL - 1], lb0, part + ((p * 3 + t - TL - 1) * 8 + tk) * 1024);
            __builtin_amdgcn_sched_barrier(0);
        }
    });
    TS(37);
    asm volatile("s_nop 7");
    *(lds_f4w)(size_t)(xw + XB_OFF - XA_OFF) = aco[0][1];
    *(lds_f4w)(size_t)(xw + XB_OFF - XA_OFF + 8192) = aco[1][1];
    asm volatile("s_waitcnt lgkmcnt(0)" ::: "memory");
    __builtin_amdgcn_s_barrier();
    asm volatile("" ::: "memory");
    f4 o0 = aco[0][0] + ldsrf(xr + XB_OFF - XA_OFF), o1 = aco[1][0] + ldsrf(xr + XB_OFF - XA_OFF + 8192);
    TS(38);
    asm volatile("s_waitcnt vmcnt(0)" : "+v"(pl[0]), "+v"(pl[1]), "+v"(pl[2]), "+v"(pl[3]), "+v"(x2v), "+v"(gb0), "+v"(gb1), "+v"(x2c), "+v"(gbc));
    TS(39);
    while (true) {
        unsigned tg = 1u;
        for (int i = 0; i < 4; i++) tg = min(tg, min(pl[i][0], pl[i][2]));
        if (__builtin_amdgcn_ballot_w64(tg == 0) == 0) break;
        __builtin_amdgcn_s_sleep(1);
        for (int i = 0; i < 3; i++) gload_sc1(pl[i], lb0, part + ((p * 3 + i) * 8 + tk) * 1024);
        gload_sc1(pl[3], off8, part);
        asm volatile("s_waitcnt vmcnt(0)" : "+v"(pl[0]), "+v"(pl[1]), "+v"(pl[2]), "+v"(pl[3]));
    }
    TS(40);
    const int q = lane >> 4;
    for (int r = 0; r < 4; r++) {
        o0[r] += (lo2f(pl[0][r]) + lo2f(pl[1][r])) + lo2f(pl[2][r]);
        o1[r] += (hi2f(pl[0][r]) + hi2f(pl[1][r])) + hi2f(pl[2][r]);
    }
    u4 ov;
    for (int r = 0; r < 4; r++) {
        unsigned ob = pkbf(o0[r], o1[r]);
        float ga = r < 2 ? gb0[2 * r] : gb1[2 * r - 4], gc = r < 2 ? gb0[2 * r + 1] : gb1[2 * r - 3];
        unsigned pb = pkbf(ga * lo2f(ob), gc * hi2f(ob));
        ov[r] = pkbf(lo2f(x2v[r]) + lo2f(pb), hi2f(x2v[r]) + hi2f(pb));
    }
    *(u4*)(out + (size_t)trow * 1152 + (nt2 * 144 + p * 32 + q * 8) * 2) = ov;
    {
        const f4 p8 = __builtin_bit_cast(f4, pl[3]);
        float u0 = p8[0], u1 = p8[1], u2 = p8[2], u3 = p8[3];
        asm volatile("s_nop 1\n\tv_permlane16_swap_b32 %0, %1\n\tv_permlane16_swap_b32 %2, %3" : "+v"(u0), "+v"(u1), "+v"(u2), "+v"(u3));
        float a = u0 + u1, c = u2 + u3;
        asm volatile("s_nop 1\n\tv_permlane32_swap_b32 %0, %1" : "+v"(a), "+v"(c));
        float o = a + c;
        *(u16*)(out + (size_t)trow * 1152 + (nt2 * 144 + 128 + p * 4 + q) * 2) = f2bf(lo2f(x2c) + rbf(__builtin_bit_cast(float, gbc) * rbf(o)));
    }
    TSFLUSH(43);
    if constexpr (R) asm volatile(".Lpfe1:");
    else asm volatile(".Lpfe0:");
}
}

__global__ NOPK __launch_bounds__(512, 2) void k_g(char* ws, char* out) {
    extern __shared__ __attribute__((aligned(16))) char smem[];
    const unsigned sm = lds_addr(smem);
    const int tid = threadIdx.x, lane = tid & 63;
    const int wid = __builtin_amdgcn_readfirstlane(tid >> 6);
    const unsigned pf = PFG ? code_pf(code_range(), wid, lane) : 0u;
    if ((wid < 4) != G_LEADHI) {
        gf::body<0>(sm, ws, out, wid, lane, pf);
        __builtin_amdgcn_endpgm();
    }
    gf::body<1>(sm, ws, out, wid, lane, pf);
}

extern "C" void kernel_launch(void* const* d_in, const int* in_sizes, int n_in,
                              void* d_out, int out_size, void* d_ws, size_t ws_size,
                              hipStream_t stream) {
    char* ws = (char*)d_ws;
    AArgs a;
    a.x = (const u16*)d_in[0];
    a.Are = (const u16*)d_in[1];
    a.Aim = (const u16*)d_in[2];
    a.Bre = (const u16*)d_in[3];
    a.Bim = (const u16*)d_in[4];
    a.Cre = (const u16*)d_in[5];
    a.Cim = (const u16*)d_in[6];
    a.logdt = (const u16*)d_in[7];
    a.c = (const u16*)d_in[13];
    a.wmod = (const u16*)d_in[14];
    a.wd = (const u16*)d_in[12];
    a.ws = ws;
    BArgs bb;
    bb.dskip = (const u16*)d_in[8];
    bb.wg = (const u16*)d_in[10];
    bb.wu = (const u16*)d_in[11];
    bb.ws = ws;
    (void)hipFuncSetAttribute((const void*)k_b, hipFuncAttributeMaxDynamicSharedMemorySize, B_SMEM);
    (void)hipFuncSetAttribute((const void*)k_g, hipFuncAttributeMaxDynamicSharedMemorySize, gf::SMEM);
    k_a<<<A_KG + A_LN + A_MOD + A_WD + A_ZERO, 256, 0, stream>>>(a);
    k_b<<<B_PF + B_CONV + B_PREP, 512, B_SMEM, stream>>>(bb);
    k_g<<<256, 512, gf::SMEM, stream>>>(ws, (char*)d_out);
}
```

```cpp
#include <hip/hip_runtime.h>
#include <hip/hip_bf16.h>
#include <stdint.h>
#include <type_traits>
#include <utility>

typedef unsigned short u16;
typedef __attribute__((ext_vector_type(8))) __bf16 bf8;
typedef __attribute__((ext_vector_type(2))) __bf16 bf2;
typedef __attribute__((ext_vector_type(2))) float f2;
typedef __attribute__((ext_vector_type(4))) float f4;
typedef __attribute__((ext_vector_type(16))) float f16v;
typedef __attribute__((ext_vector_type(4))) unsigned u4;
typedef __attribute__((ext_vector_type(2))) unsigned u2;

#define L_ 2048
#define D_ 576
#define N_ 128
#define F_ 4608

#define WS_MODP   0u
#define WS_MODF   (128u << 10)
#define WS_FLAG   (160u << 10)
#define WS_SD     (164u << 10)
#define WS_U12    (192u << 10)
#define WS_STAT   (176u << 10)
#define WS_XQ     (1u << 20)
#define WS_KR     (4u << 20)
#define WS_X2O    (8u << 20)
#define WS_W1     (12u << 20)
#define WS_WD     (24u << 20)
#define WS_H      (32u << 20)
#define WS_PART   (64u << 20)
#define WS_TMP    (96u << 20)
#define STREAM_B  (41 * 8192)

__device__ __forceinline__ float bf2f(u16 v) { return __uint_as_float(((unsigned)v) << 16); }
__device__ __forceinline__ u16 f2bf(float f) {
    __bf16 b = (__bf16)f;
    return __builtin_bit_cast(u16, b);
}
__device__ __forceinline__ float rbf(float f) { return bf2f(f2bf(f)); }
__device__ __forceinline__ unsigned pkbf(float a, float b) {
    f2 p = {a, b};
    return __builtin_bit_cast(unsigned, __builtin_convertvector(p, bf2));
}

template <int B, class F, int... I>
__device__ __forceinline__ void sf_impl(F&& f, std::integer_sequence<int, I...>) {
    (f(std::integral_constant<int, B + I>{}), ...);
}
template <int I, int N, class F>
__device__ __forceinline__ void static_for(F&& f) {
    if constexpr (N - I > 128) {
        static_for<I, I + 128>(f);
        static_for<I + 128, N>(f);
    } else if constexpr (I < N) sf_impl<I>(f, std::make_integer_sequence<int, N - I>{});
}
template <int N>
__device__ __forceinline__ void wait_vm() {
    asm volatile("s_waitcnt vmcnt(%0)" ::"n"(N > 63 ? 63 : N) : "memory");
}
__device__ __forceinline__ void gload16(u4& r, unsigned voff, const char* base) {
    asm volatile("global_load_dwordx4 %0, %1, %2" : "=v"(r) : "v"(voff), "s"(base));
}
__device__ __forceinline__ void glds16(unsigned lds, unsigned voff, const char* base) {
    asm volatile("s_mov_b32 m0, %0\n\ts_nop 0\n\tglobal_load_lds_dwordx4 %1, %2" ::"s"(lds), "v"(voff), "s"(base) : "memory");
}
typedef __attribute__((address_space(3))) const u4* lds_u4p;
typedef __attribute__((address_space(3))) const f4* lds_f4p;
__device__ __forceinline__ u4 ldsr(unsigned a) { return *(lds_u4p)(size_t)a; }
__device__ __forceinline__ f4 ldsrf(unsigned a) { return *(lds_f4p)(size_t)a; }
__device__ __forceinline__ unsigned lds_addr(char* p) {
    return (unsigned)(size_t)(__attribute__((address_space(3))) char*)p;
}
__device__ __forceinline__ void mfma(f4& c, const u4& a, const u4& b) {
    asm volatile("v_mfma_f32_16x16x32_bf16 %0, %1, %2, %0" : "+v"(c) : "v"(a), "v"(b));
}
__device__ __forceinline__ void mfma0(f4& c, const u4& a, const u4& b) {
    asm volatile("v_mfma_f32_16x16x32_bf16 %0, %1, %2, 0" : "=&v"(c) : "v"(a), "v"(b));
}
template <int N>
__device__ __forceinline__ void wait_vm_reg(u4& r) {
    asm volatile("s_waitcnt vmcnt(%1)" : "+v"(r) : "n"(N > 63 ? 63 : N));
}

__host__ __device__ inline int w1_col(int c18, int np, int r) { return 288 * np + 96 * (c18 / 6) + 6 * r + c18 % 6; }
__host__ __device__ inline int h_col(int s, int q, int e) {
    int np = s / 9, r9 = s % 9;
    int c18 = r9 < 8 ? 9 * (r9 >> 2) + 2 * (r9 & 3) + e : 9 * e + 8;
    return w1_col(c18, np, 4 * q);
}
__host__ __device__ constexpr int g2_pos(int s) {
    int np = s / 9, r9 = s % 9;
    return r9 == 8 ? 16 + np : (r9 & 2) ? 8 + (r9 & 1) * 4 + np * 2 + (r9 >> 2) : np * 4 + (r9 >> 2) * 2 + (r9 & 1);
}
__host__ __device__ constexpr int g2_inv(int k) {
    return k >= 16 ? (k - 16) * 9 + 8 : k < 8 ? (k >> 2) * 9 + ((k >> 1) & 1) * 4 + (k & 1) : ((k >> 1) & 1) * 9 + (k & 1) * 4 + 2 + ((k - 8) >> 2);
}
__host__ __device__ inline int g2_slot(int p, int ch, int ks) {
    if (ch == 8) return ks < 12 ? ks * 7 : 48 + ks * 3;
    int g = ((ch >> 1) - p - 1) & 3;
    if (g == 3) return 126 + ks * 2 + (ch & 1);
    if (ks < 12) return ks * 7 + 1 + g * 2 + (ch & 1);
    return g == 0 ? 49 + ks * 3 + (ch & 1) : 66 + g * 12 + ks * 2 + (ch & 1);
}
__device__ __forceinline__ float lo2f(unsigned u) { return __builtin_bit_cast(float, u << 16); }
__device__ __forceinline__ float hi2f(unsigned u) { return __builtin_bit_cast(float, u & 0xffff0000u); }
__device__ __forceinline__ void st_sc1(void* p, u4 v) {
    asm volatile("global_store_dwordx4 %0, %1, off sc1\n\ts_nop 1" ::"v"(p), "v"(v) : "memory");
}
template <int CTRL>
__device__ __forceinline__ float dppf(float v) {
    return __builtin_bit_cast(float, __builtin_amdgcn_update_dpp(0, __builtin_bit_cast(int, v), CTRL, 0xf, 0xf, true));
}
__device__ __forceinline__ float row_sum(float v) {
    v += dppf<0xb1>(v); v += dppf<0x4e>(v); v += dppf<0x141>(v); v += dppf<0x140>(v);
    return v;
}
__device__ __forceinline__ float wave_max(float v) {
    v = fmaxf(v, dppf<0xb1>(v)); v = fmaxf(v, dppf<0x4e>(v)); v = fmaxf(v, dppf<0x141>(v)); v = fmaxf(v, dppf<0x140>(v));
    int iv = __builtin_bit_cast(int, v);
    float a = __builtin_bit_cast(float, __builtin_amdgcn_readlane(iv, 0)), b = __builtin_bit_cast(float, __builtin_amdgcn_readlane(iv, 16));
    float c = __builtin_bit_cast(float, __builtin_amdgcn_readlane(iv, 32)), d = __builtin_bit_cast(float, __builtin_amdgcn_readlane(iv, 48));
    return fmaxf(fmaxf(a, b), fmaxf(c, d));
}
typedef __attribute__((ext_vector_type(2))) _Float16 h2;
typedef __attribute__((ext_vector_type(8))) _Float16 h8;
__device__ __forceinline__ f2 cmul(f2 a, f2 b) {
    return __builtin_elementwise_fma(f2{a[0], a[0]}, b, f2{a[1], a[1]} * f2{-b[1], b[0]});
}
__device__ __forceinline__ f2 cpow(float e2, float rev, float n) {
    float m = __builtin_amdgcn_exp2f(e2 * n), r = __builtin_amdgcn_fractf(rev * n);
    return f2{m * __builtin_amdgcn_cosf(r), m * __builtin_amdgcn_sinf(r)};
}
__device__ __forceinline__ unsigned pkh(f2 v) { return __builtin_bit_cast(unsigned, __builtin_convertvector(v, h2)); }

struct CodeRange {
    const char* pc;
    int len;
};
__device__ __forceinline__ CodeRange code_range() {
    CodeRange c;
    asm volatile("s_getpc_b64 %0\n2:\n\ts_mov_b32 %1, .Lpfe0-2b\n\ts_max_i32 %1, %1, .Lpfe1-2b\n\ts_max_i32 %1, %1, 0" : "=s"(c.pc), "=s"(c.len)::"scc");
    return c;
}
__device__ __forceinline__ unsigned code_pf(const CodeRange& c, int chunk, int lane) {
    unsigned r;
    asm volatile("global_load_dword %0, %1, %2" : "=v"(r) : "v"(min(chunk * 8192 + lane * 128, c.len)), "s"(c.pc));
    return r;
}
#define PFA 1
#define PFB 1
#define PFG 1
#define A_KG 288
#define A_LN 128
#define A_MOD 162
#define A_WD 144
#define A_ZERO 8
#define A_SMEM (4 * 2 * 4352 + 2 * 5184)
struct AArgs {
    const u16 *x, *Are, *Aim, *Bre, *Bim, *Cre, *Cim, *logdt, *c, *wmod, *wd;
    char* ws;
};
__global__ __launch_bounds__(256) void k_a(AArgs A) {
    __shared__ __attribute__((aligned(16))) char smem[A_SMEM];
    const CodeRange cr = code_range();
    int b = blockIdx.x;
    const int tid = threadIdx.x, lane = tid & 63, wid = __builtin_amdgcn_readfirstlane(tid >> 6);
    const int q = lane >> 4, i = lane & 15;
    if (b < A_ZERO) {
        unsigned r[3];
        if (PFA) for (int k = 0; k < 3; k++) r[k] = code_pf(cr, k * 4 + wid, lane);
        if (b == 0)
            for (int k = 0; k < 3; k++) *(u4*)(A.ws + WS_FLAG + (k * 256 + tid) * 16) = u4{0, 0, 0, 0};
        if (PFA) asm volatile("s_waitcnt vmcnt(0)" ::"v"(r[0]), "v"(r[1]), "v"(r[2]));
        return;
    }
    b -= A_ZERO;
    if (b < A_KG) {
        __builtin_amdgcn_s_setprio(3);
        const int d = b * 2 + (wid >> 1), n = d * N_ + (wid & 1) * 64 + lane;
        const u16 l0_ = A.Are[n], l1_ = A.Aim[n], l2_ = A.Bre[n], l3_ = A.Bim[n], l4_ = A.Cre[n], l5_ = A.Cim[n], l6_ = A.logdt[d];
        const float are = bf2f(l0_), aim = bf2f(l1_), bre = bf2f(l2_), bim = bf2f(l3_), cre = bf2f(l4_), cim = bf2f(l5_), ldt = bf2f(l6_);
        const float dt = rbf(__builtin_amdgcn_exp2f(ldt * 1.44269504f));
        const float ar = dt * are * 1.44269504f, rev = dt * aim * 0.159154943f;
        f2 w = cmul(f2{cre, cim}, f2{dt * bre, dt * bim});
        const float amax = wave_max(ar);
        float wmax = wave_max(fmaxf(fabsf(w[0]), fabsf(w[1])));
        wmax = wmax > 1e-30f ? wmax : 1.f;
        w *= __builtin_amdgcn_rcpf(wmax);
        const float e2 = ar - amax;
        const f2 z1 = cpow(e2, rev, 1.f), z8 = cpow(e2, rev, 8.f), z128 = cpow(e2, rev, 128.f);
        unsigned* buf0 = (unsigned*)(smem + wid * 8704);
        unsigned* buf1 = buf0 + 1088;
        const h8* rd0 = (const h8*)(buf0 + i * 68 + q * 4);
        const h8* rd1 = (const h8*)(buf1 + i * 68 + q * 4);
        {
            const f2 z8c = {z8[0], -z8[1]};
            f2 v = z8c;
            buf0[lane] = pkh(f2{1.f, 0.f});
            for (int c = 1; c < 16; c++) { buf0[c * 68 + lane] = pkh(v); v = cmul(v, z8c); }
        }
        f2 zp[8];
        zp[1] = z1;
        for (int r = 2; r < 8; r++) zp[r] = cmul(zp[r - 1], z1);
        f2 g = w;
        for (int k = 0; k < 2; k++) {
            buf1[(k * 8) * 68 + lane] = pkh(g);
            for (int r = 1; r < 8; r++) buf1[(k * 8 + r) * 68 + lane] = pkh(cmul(g, zp[r]));
            g = cmul(g, z128);
        }
        h8 vf[4];
        f4 acc[3];
        for (int k = 0; k < 4; k++) vf[k] = rd0[k * 4];
        acc[0] = f4{0.f, 0.f, 0.f, 0.f};
        for (int k = 0; k < 4; k++) acc[0] = __builtin_amdgcn_mfma_f32_16x16x32_f16(rd1[k * 4], vf[k], acc[0], 0, 0, 0);
        for (int k = 0; k < 2; k++) {
            buf0[(k * 4) * 68 + lane] = pkh(g);
            for (int r = 1; r < 4; r++) buf0[(k * 4 + r) * 68 + lane] = pkh(cmul(g, zp[2 * r]));
            g = cmul(g, z128);
        }
        for (int k = 0; k < 4; k++) {
            buf0[(8 + k * 2) * 68 + lane] = pkh(g);
            buf0[(9 + k * 2) * 68 + lane] = pkh(cmul(g, zp[4]));
            g = cmul(g, z128);
        }
        acc[1] = f4{0.f, 0.f, 0.f, 0.f};
        for (int k = 0; k < 4; k++) acc[1] = __builtin_amdgcn_mfma_f32_16x16x32_f16(rd0[k * 4], vf[k], acc[1], 0, 0, 0);
        for (int k = 0; k < 9; k++) {
            buf1[k * 68 + lane] = pkh(g);
            g = cmul(g, z128);
        }
        acc[2] = f4{0.f, 0.f, 0.f, 0.f};
        for (int k = 0; k < 4; k++) acc[2] = __builtin_amdgcn_mfma_f32_16x16x32_f16(rd1[k * 4], vf[k], acc[2], 0, 0, 0);
        float* T = (float*)(smem + 4 * 8704 + (wid >> 1) * 5184);
        for (int r = 0; r < 4; r++) {
            int u0 = 128 * (q >> 1) + 4 * (q & 1) + r + 8 * i;
            T[u0 * 2 + (wid & 1)] = acc[0][r] * (wmax * __builtin_amdgcn_exp2f(amax * (float)u0));
            int u1 = q < 2 ? 256 + 128 * q + 2 * r : 256 * q + 128 * (r >> 1) + 4 * (r & 1);
            int i1 = q < 2 ? 256 + 64 * q + r + 4 * i : 256 + 64 * q + 32 * (r >> 1) + (r & 1) + 2 * i;
            T[i1 * 2 + (wid & 1)] = acc[1][r] * (wmax * __builtin_amdgcn_exp2f(amax * (float)(u1 + 8 * i)));
            int i2 = 64 * q + 16 * r + i;
            if (i2 <= 128) T[(512 + i2) * 2 + (wid & 1)] = acc[2][r] * (wmax * __builtin_amdgcn_exp2f(amax * (float)(1024 + 8 * i2)));
        }
        __syncthreads();
        const f2* T2 = (const f2*)T;
        auto tv = [&](int idx) { f2 t = T2[idx]; return t[0] + t[1]; };
        char* ke = A.ws + WS_KR + d * 3072;
        const int l128 = tid & 127;
        *(unsigned*)(ke + l128 * 4) = pkbf(tv(639 - l128), tv(640 - l128));
        if ((wid & 1) == 0) {
            int i0 = 510 - 2 * lane;
            *(u2*)(ke + 512 + lane * 8) = u2{pkbf(tv(i0), tv(i0 + 1)), pkbf(tv(i0 + 2), 0.f)};
        } else {
            float v[9];
            const int l0 = 511 - 8 * lane;
            for (int j = 0; j < 9; j++) {
                int l = l0 - j;
                int hf = l >> 1;
                int idx = l < 256 ? (l < 0 ? 0 : l) : 128 + hf + (l & hf & 1);
                v[j] = tv(idx);
            }
            if (lane == 63) v[8] = 0.f;
            *(u4*)(ke + 1024 + lane * 16) = u4{pkbf(v[1], v[2]), pkbf(v[3], v[4]), pkbf(v[5], v[6]), pkbf(v[7], v[8])};
            *(u4*)(ke + 2048 + lane * 16) = u4{pkbf(v[0], v[1]), pkbf(v[2], v[3]), pkbf(v[4], v[5]), pkbf(v[6], v[7])};
        }
        return;
    }
    b -= A_KG;
    if (b < A_LN) {
        const int tok = tid >> 4;
        const u2* xr = (const u2*)(A.x + (size_t)(b * 16 + tok) * D_) + i;
        u2 v[9];
        for (int k = 0; k < 9; k++) v[k] = xr[16 * k];
        float s = 0.f;
        for (int k = 0; k < 9; k++) {
            *(u2*)(smem + (i + 16 * k) * 144 + tok * 8) = v[k];
            s += (lo2f(v[k][0]) + hi2f(v[k][0])) + (lo2f(v[k][1]) + hi2f(v[k][1]));
        }
        const float mean = row_sum(s) * (1.f / D_);
        float s2 = 0.f;
        for (int k = 0; k < 9; k++)
            for (int e = 0; e < 2; e++) {
                float d0 = lo2f(v[k][e]) - mean, d1 = hi2f(v[k][e]) - mean;
                s2 += d0 * d0 + d1 * d1;
            }
        const float var = row_sum(s2) * (1.f / D_);
        if (i == 0) *(unsigned*)(A.ws + WS_STAT + (b * 16 + tok) * 4) = pkbf(mean, __builtin_sqrtf(rbf(rbf(var) + 1e-5f)));
        __syncthreads();
        for (int k = 0; k < 5; k++) {
            int g = k * 256 + tid;
            if (g < 1152) st_sc1(A.ws + WS_XQ + (g >> 3) * 16384 + b * 128 + (g & 7) * 16, *(const u4*)(smem + (g >> 3) * 144 + (g & 7) * 16));
        }
        return;
    }
    b -= A_LN;
    if (b < A_MOD) {
        const int cb = b % 54, kp = b / 54, cg = tid & 7, rg = tid >> 3;
        u4 wv[6];
        float sv[6];
        for (int k = 0; k < 6; k++) {
            int row = kp * 192 + k * 32 + rg;
            wv[k] = *(const u4*)(A.wmod + (size_t)row * (6 * D_) + cb * 64 + cg * 8);
            sv[k] = bf2f(A.c[row]);
        }
        float a[8] = {};
        for (int k = 0; k < 6; k++) {
            float s = rbf(sv[k] * __builtin_amdgcn_rcpf(1.f + __builtin_amdgcn_exp2f(-1.44269504f * sv[k])));
            for (int e = 0; e < 4; e++) { a[2 * e] += s * lo2f(wv[k][e]); a[2 * e + 1] += s * hi2f(wv[k][e]); }
        }
        float* red = (float*)smem;
        *(f4*)(red + rg * 68 + cg * 8) = f4{a[0], a[1], a[2], a[3]};
        *(f4*)(red + rg * 68 + cg * 8 + 4) = f4{a[4], a[5], a[6], a[7]};
        __syncthreads();
        const int col = tid >> 2, part = tid & 3;
        float s = 0.f;
        for (int k = 0; k < 8; k++) s += red[(part * 8 + k) * 68 + col];
        s += dppf<0xb1>(s);
        s += dppf<0x4e>(s);
        if (part == 0) ((float*)(A.ws + WS_MODP))[kp * 3456 + cb * 64 + col] = s;
        return;
    }
    b -= A_MOD;
    {
        const int kc = b / 36, kh = (b % 36) / 18, ks = g2_pos(b % 18);
        const u16* r0 = A.wd + (size_t)h_col(b, q, 0) * D_ + wid * 144;
        const u16* r1 = A.wd + (size_t)h_col(b, q, 1) * D_ + wid * 144;
        unsigned v[5][8];
        for (int P = 0; P < 5; P++)
            for (int j = 0; j < 8; j++)
                v[P][j] = *(const unsigned*)((j < 4 ? r0 : r1) + (j & 3) * 6 * D_ + P * 32 + (P < 4 ? 2 * i : (i & 14)));
        for (int j = 0; j < 8; j++) asm volatile("" : "+v"(v[4][j]));
        char* dst = A.ws + WS_WD + (size_t)(wid * 4 + kc) * STREAM_B + kh * 1024 + lane * 16;
        for (int P = 0; P < 4; P++)
            for (int e = 0; e < 2; e++) {
                u4 o;
                for (int j = 0; j < 4; j++) o[j] = __builtin_amdgcn_perm(v[P][2 * j + 1], v[P][2 * j], e ? 0x07060302u : 0x05040100u);
                st_sc1(dst + g2_slot(kc, 2 * P + e, ks) * 2048, o);
            }
        u4 o;
        const unsigned sel = (i & 1) ? 0x07060302u : 0x05040100u;
        for (int j = 0; j < 4; j++) o[j] = __builtin_amdgcn_perm(v[4][2 * j + 1], v[4][2 * j], sel);
        st_sc1(dst + g2_slot(kc, 8, ks) * 2048, o);
    }
}

#define B_CONV 144
#define B_PREP 96
#define B_PF (PFB ? 8 : 0)
#define B_HP 0
#define B_H2 (16640 + 124)
#define B_R  (2 * 16640 + 128)
#define B_Y  (B_R + 4 * 9152)
#define B_SMEM (B_Y + 65536)
struct BArgs {
    const u16 *dskip, *wg, *wu;
    char* ws;
};
__global__ __launch_bounds__(512, 2) void k_b(BArgs A) {
    extern __shared__ __attribute__((aligned(16))) char smem[];
    const CodeRange cr = code_range();
    int b = blockIdx.x;
    const int tid = threadIdx.x, lane = tid & 63, wid = __builtin_amdgcn_readfirstlane(tid >> 6);
    const int q = lane >> 4, i = lane & 15;
    const float* modp = (const float*)(A.ws + WS_MODP);
    if (b < B_PF) {
        const unsigned r0 = code_pf(cr, wid, lane);
        asm volatile("s_waitcnt vmcnt(0)" ::"v"(r0));
        return;
    }
    b -= B_PF;
    if (b < B_CONV) {
        const int Q = (b >> 4) * 16 + (b & 7) * 2 + ((b >> 3) & 1);
        const unsigned sm = lds_addr(smem);
        const char* ke = A.ws + WS_KR + Q * 4 * 3072;
        glds16(sm + B_R + (wid >> 1) * 9152 + (wid & 1) * 4576 + 3072, lane * 16, ke + (wid >> 1) * 3072 + 1024 + (wid & 1) * 1024);
        const u4* xp = (const u4*)(A.ws + WS_XQ + Q * 16384 + tid * 32);
        const u4 xa = xp[0], xb = xp[1];
        const u4 st = *(const u4*)(A.ws + WS_STAT + tid * 16);
        const unsigned e3 = *(const unsigned*)(ke + (tid >> 7) * 3072 + (tid & 127) * 4);
        const u2 e2 = *(const u2*)(ke + (tid >> 7) * 3072 + 512 + lane * 8);
        float sa[4], c1[4], ga[4], dk[4];
        for (int c = 0; c < 4; c++) {
            const float* m = modp + Q * 4 + c;
            sa[c] = rbf(m[0] + m[3456] + m[6912]);
            c1[c] = rbf(1.f + rbf(m[576] + m[3456 + 576] + m[6912 + 576]));
            ga[c] = rbf(m[1152] + m[3456 + 1152] + m[6912 + 1152]);
            dk[c] = bf2f(A.dskip[Q * 4 + c]);
        }
        if (lane < 28) *(u4*)(smem + B_R + (wid >> 1) * 9152 + (wid & 1) * 4576 + 4096 + lane * 16) = u4{0, 0, 0, 0};
        else if (lane >= 48) *(unsigned*)(smem + (wid >> 2) * B_H2 + (wid & 3) * 4160 + (lane - 48) * 4) = 0u;
        {
            char* r = smem + B_R + (tid >> 7) * 9152;
            const unsigned aa = __builtin_amdgcn_perm(e3, e3, 0x01000100u), bb = __builtin_amdgcn_perm(e3, e3, 0x03020302u);
            const unsigned ba = __builtin_amdgcn_perm(e3, e3, 0x01000302u);
            const bool modd = (tid & 1) == 0;
            *(u4*)(r + (tid & 127) * 16) = u4{bb, modd ? ba : aa, aa, aa};
            *(u4*)(r + 4576 + (tid & 127) * 16) = u4{bb, modd ? bb : ba, aa, aa};
            const unsigned t00 = __builtin_amdgcn_perm(e2[0], e2[0], 0x01000100u), t11 = __builtin_amdgcn_perm(e2[0], e2[0], 0x03020302u);
            const unsigned s0 = (wid & 1) ? 0x05040504u : 0x03020504u, s2 = (wid & 1) ? 0x01000302u : 0x01000100u;
            *(u4*)(r + (wid & 1) * 4576 + 2048 + lane * 16) =
                u4{__builtin_amdgcn_perm(e2[1], e2[0], s0), t11, __builtin_amdgcn_perm(e2[0], e2[0], s2), t00};
        }
        unsigned hd[4][2];
        {
            float hv[4][4];
            for (int t = 0; t < 4; t++) {
                const float m = lo2f(st[t]), rs = __builtin_amdgcn_rcpf(hi2f(st[t]));
                for (int e = 0; e < 2; e++) {
                    const unsigned xw = t < 2 ? xa[2 * t + e] : xb[2 * t - 4 + e];
                    unsigned u = pkbf(lo2f(xw) - m, hi2f(xw) - m);
                    u = pkbf(lo2f(u) * rs, hi2f(u) * rs);
                    u = pkbf(lo2f(u) * c1[2 * e], hi2f(u) * c1[2 * e + 1]);
                    hv[2 * e][t] = lo2f(u) + sa[2 * e];
                    hv[2 * e + 1][t] = hi2f(u) + sa[2 * e + 1];
                }
            }
            for (int c = 0; c < 4; c++) {
                u2 h = {pkbf(hv[c][0], hv[c][1]), pkbf(hv[c][2], hv[c][3])};
                *(u2*)(smem + B_HP + c * 4160 + 64 + tid * 8) = h;
                for (int e = 0; e < 2; e++) *(unsigned*)(smem + B_H2 + c * 4160 + 64 + tid * 8 + e * 4) = h[e];
                for (int e = 0; e < 2; e++) hd[c][e] = pkbf(lo2f(h[e]) * dk[c], hi2f(h[e]) * dk[c]);
            }
        }
        wait_vm<0>();
        __syncthreads();
        {
            const int ch = wid >> 1, hf = wid & 1;
            const unsigned ab = sm + (i & 1 ? B_HP : B_H2) + ch * 4160 + (hf * 64 + 4 * q + i + 1) * 4;
            const unsigned bbase = sm + B_R + ch * 9152 + (i & 1) * 4576 + 448 - 64 * (i >> 1) + 256 * hf + 16 * q;
            f4 acc[8];
            for (int c = 0; c < 8; c++) acc[c] = f4{0.f, 0.f, 0.f, 0.f};
            typedef __attribute__((address_space(3))) const volatile u2* lds_u2p;
            u4 bf[2][8], af[3][2];
            static_for<-6, 16>([&](auto SI) {
                constexpr int s = decltype(SI)::value, rho = s >> 2, m = s & 3;
                if constexpr (s + 2 >= 0 && s + 2 < 16)
                    for (int e = 0; e < 2; e++) {
                        const unsigned o = ab + (e ? (s + 2) & 3 : 7 - ((s + 2) & 3)) * 512 + ((s + 2) >> 2) * 64;
                        const u2 l = *(lds_u2p)(size_t)o, h = *(lds_u2p)(size_t)(o + 8);
                        af[(s + 2) % 3][e] = u4{l[0], l[1], h[0], h[1]};
                    }
                constexpr int sb = s < 0 ? s + 6 : s + 4;
                if constexpr (sb < 16 && (s >= 0 || sb < 4))
                    for (int e = 0; e < 2; e++) bf[(sb >> 2) & 1][(sb & 3) * 2 + e] = ldsr(bbase + (7 - (sb & 3) * 2 - e) * 512 + (sb >> 2) * 64);
                if constexpr (s >= 0) {
                    for (int k = 0; k <= m; k++)
                        acc[7 - m + k] = __builtin_amdgcn_mfma_f32_16x16x32_bf16(__builtin_bit_cast(bf8, af[s % 3][0]), __builtin_bit_cast(bf8, bf[rho & 1][k]), acc[7 - m + k], 0, 0, 0);
                    for (int k = 0; k + m < 8; k++)
                        acc[m + k] = __builtin_amdgcn_mfma_f32_16x16x32_bf16(__builtin_bit_cast(bf8, af[s % 3][1]), __builtin_bit_cast(bf8, bf[rho & 1][k]), acc[m + k], 0, 0, 0);
                    for (int k = 0; k < 6; k++) {
                        __builtin_amdgcn_sched_group_barrier(0x008, 1, 0);
                        __builtin_amdgcn_sched_group_barrier(0x100, 1, 0);
                    }
                }
                __builtin_amdgcn_sched_barrier(0);
            });
            char* yw = smem + B_Y + hf * 32768 + ch * 8192 + (i & 1) * 4096 + (4 * (i >> 1) + (q ^ ((i >> 1 & 2) + (i & 1)))) * 16;
            for (int c = 0; c < 8; c++) *(f4*)(yw + c * 512) = acc[c];
        }
        __syncthreads();
        float x2[4][4];
        for (int c = 0; c < 4; c++) {
            const char* yr = smem + B_Y + c * 8192 + ((tid ^ (tid >> 2 & 4)) * 8);
            const char* ys = smem + B_Y + c * 8192 + 4096 + ((tid ^ (tid >> 2 & 4) ^ 2) * 8);
            const f2 y0 = *(const f2*)yr + *(const f2*)(yr + 32768), y1 = *(const f2*)ys + *(const f2*)(ys + 32768);
            for (int e = 0; e < 2; e++) {
                unsigned u = pkbf(y0[e], y1[e]);
                u = pkbf(lo2f(u) + lo2f(hd[c][e]), hi2f(u) + hi2f(hd[c][e]));
                u = pkbf(lo2f(u) * ga[c], hi2f(u) * ga[c]);
                const unsigned x0 = e ? xb[c >> 1] : xa[c >> 1], x1 = e ? xb[2 + (c >> 1)] : xa[2 + (c >> 1)];
                x2[2 * e][c] = ((c & 1) ? hi2f(x0) : lo2f(x0)) + lo2f(u);
                x2[2 * e + 1][c] = ((c & 1) ? hi2f(x1) : lo2f(x1)) + hi2f(u);
            }
        }
        char* xo = A.ws + WS_X2O + (Q >> 1) * 32768 + tid * 64 + (Q & 1) * 8;
        for (int t = 0; t < 4; t++) *(u2*)(xo + t * 16) = u2{pkbf(x2[t][0], x2[t][1]), pkbf(x2[t][2], x2[t][3])};
        return;
    }
    b -= B_CONV;
    {
        const int up = b / 48, np = (b & 7) * 2 + (b >> 3 & 1), un = (b >> 4) % 3;
        const char* src = (const char*)((up ? A.wu : A.wg) + (size_t)(wid * 32 + q * 8) * F_ + 288 * np + 96 * un + 6 * i);
        typedef __attribute__((ext_vector_type(3))) unsigned u3;
        typedef u3 __attribute__((aligned(4))) u3a;
        float* tc = (float*)smem;
        float* tsb = tc + 576;
        float* red = tc + 1152;
        const f2* m = (const f2*)(modp + 3 * D_) + (tid < 288 ? tid : 0);
        f2 mv[9];
        for (int j = 0; j < 9; j++) mv[j] = m[(j % 3) * 1728 + (j / 3) * 288];
        __builtin_amdgcn_sched_barrier(0);
        u3 v[3][8];
        for (int k = 0; k < 2; k++)
            for (int j = 0; j < 8; j++) v[k][j] = *(const u3a*)(src + (size_t)(k * 256 + j) * (F_ * 2));
        if (wid < 2)
            for (int j = 0; j < 8; j++) v[2][j] = *(const u3a*)(src + (size_t)(512 + j) * (F_ * 2));
        __builtin_amdgcn_sched_barrier(0);
        if (tid < 288) {
            const f2 sb = mv[0] + mv[1] + mv[2], cb = mv[3] + mv[4] + mv[5], gb = mv[6] + mv[7] + mv[8];
            *(f2*)(tc + 2 * tid) = f2{rbf(1.f + rbf(cb[0])), rbf(1.f + rbf(cb[1]))};
            *(f2*)(tsb + 2 * tid) = f2{rbf(sb[0]), rbf(sb[1])};
            if (b == 0) *(f2*)(A.ws + WS_MODF + (5 * D_ + 2 * tid) * 4) = f2{rbf(gb[0]), rbf(gb[1])};
        }
        asm volatile("s_waitcnt lgkmcnt(0)\n\ts_barrier" ::: "memory");
        float s1[6] = {}, s2[6] = {};
        auto pass1 = [&](int k) {
            const int k0 = (wid + 8 * k) * 32 + q * 8;
            for (int j = 0; j < 8; j++) {
                const float c = tc[k0 + j], s = tsb[k0 + j];
                for (int e = 0; e < 3; e++) {
                    const float wl = lo2f(v[k][j][e]), wh = hi2f(v[k][j][e]);
                    s1[2 * e] += wl * c; s2[2 * e] += wl * s;
                    s1[2 * e + 1] += wh * c; s2[2 * e + 1] += wh * s;
                }
            }
        };
        pass1(0); pass1(1);
        if (wid < 2) pass1(2);
        {
            float t[6];
            for (int e = 0; e < 3; e++) {
                asm volatile("s_nop 1\n\tv_permlane16_swap_b32 %0, %1\n\tv_permlane16_swap_b32 %2, %3" : "+v"(s1[e]), "+v"(s1[3 + e]), "+v"(s2[e]), "+v"(s2[3 + e]));
                t[e] = s1[e] + s1[3 + e];
                t[3 + e] = s2[e] + s2[3 + e];
            }
            for (int e = 0; e < 3; e++) {
                asm volatile("s_nop 1\n\tv_permlane32_swap_b32 %0, %1" : "+v"(t[e]), "+v"(t[3 + e]));
                red[((3 * q + e) * 16 + i) * 8 + wid] = t[e] + t[3 + e];
            }
        }
        __syncthreads();
        float mean[6];
        for (int c = 0; c < 6; c++) {
            const f4 a = *(const f4*)(red + (c * 16 + i) * 8), bq = *(const f4*)(red + (c * 16 + i) * 8 + 4);
            const f4 s = a + bq;
            mean[c] = ((s[0] + s[1]) + (s[2] + s[3])) * (1.f / D_);
        }
        if (tid < 96) {
            const f4 a = *(const f4*)(red + (96 + tid) * 8), bq = *(const f4*)(red + (96 + tid) * 8 + 4);
            const f4 s = a + bq;
            const int c18 = un * 6 + (tid >> 4);
            *(float*)(A.ws + WS_U12 + (2 * np + c18 / 9) * 2048 + ((c18 % 9) * 32 + up * 16 + (tid & 15)) * 4) = (s[0] + s[1]) + (s[2] + s[3]);
        }
        auto pass2 = [&](int k) {
            const int ks = wid + 8 * k, k0 = ks * 32 + q * 8;
            float c[8];
            for (int j = 0; j < 8; j++) { c[j] = tc[k0 + j]; asm volatile("" : "+v"(v[k][j])); }
            for (int cc = 0; cc < 6; cc++) {
                u4 o;
                for (int j = 0; j < 4; j++) {
                    const unsigned w0 = v[k][2 * j][cc >> 1], w1 = v[k][2 * j + 1][cc >> 1];
                    o[j] = pkbf(((cc & 1) ? hi2f(w0) : lo2f(w0)) * c[2 * j] - mean[cc], ((cc & 1) ? hi2f(w1) : lo2f(w1)) * c[2 * j + 1] - mean[cc]);
                }
                const int c18 = un * 6 + cc, nt = 2 * np + c18 / 9, ch = c18 % 9;
                const int fi = ch < 4 ? ks * 8 + ch * 2 + up : ch < 8 ? (ch >> 1) * 72 + ks * 4 + (ch & 1) * 2 + up : 288 + ks * 2 + up;
                st_sc1(A.ws + WS_W1 + (size_t)nt * STREAM_B + fi * 1024 + lane * 16, o);
            }
        };
        pass2(0); pass2(1);
        if (wid < 2) pass2(2);
    }
}

#if defined(__HIP_DEVICE_COMPILE__)
#define NOPK __attribute__((target("no-packed-fp32-ops")))
#else
#define NOPK
#endif
#define G_WT 32
#define G_WU 32
#define G_WU2 56
#define G_HS 16
#define G_TZ0 44
#define G_TZS 24
#define G_LK 3
#define G_HE 6
#define G_TSL -16
#define G_TST 12
#define G_UE 23
#define G_PACE 5
#define G_URG 36
#define G_TL 150
#define G_PRIO 1
#define G_LEADHI 1
namespace gf {
constexpr int NS1 = 324, NS = 486, LAG = 32, NU = 82, RING = 19, WT = G_WT, WU = G_WU, TSD = 110;
constexpr int NSYNC = 26, LK = G_LK;
constexpr int PP[2][NSYNC + 1] = {
    {0, 8, 16, 32, 48, 64, 80, 104, 128, 176, 192, 208, 232, 256, 280, 304, 322, 340, 356, 372, 392, 412, 428, 440, 452, 468, 486},
    {-32, -24, -16, 0, 16, 32, 48, 72, 96, 144, 168, 192, 216, 240, 264, 288, 308, 324, 348, 372, 392, 412, 428, 440, 452, 468, 486}};
constexpr int JFLAG[4] = {12, 14, 16, 18};
constexpr int TPL[2][4] = {{248, 300, 342, 374}, {234, 292, 330, 374}};
constexpr int TPC[2][4] = {{260, 310, 352, 384}, {246, 302, 340, 384}};
constexpr int TEP[4] = {143, 215, 287, 323};
constexpr int THS[9] = {143, 143 + G_HS, 143 + 2 * G_HS, 143 + 3 * G_HS, 215, 215 + G_HS, 287, 287, 323};
constexpr int TZ0 = G_TZ0, TZS = G_TZS, TSTAT = G_TST;
constexpr int isflag(int j) { return j == JFLAG[0] ? 0 : j == JFLAG[1] ? 1 : j == JFLAG[2] ? 2 : j == JFLAG[3] ? 3 : -1; }
constexpr int TX[3] = {NS1 + 104, NS1 + 116, NS1 + 128}, TMISC = NS1 + 134, TL = NS1 + G_TL;
constexpr int hneed(int ks) { return NS1 + (ks < 12 ? ks * 7 : 48 + ks * 3); }
constexpr int ufirst(int u) { return u < 41 ? 8 * u : NS1 + 4 * (u - 41); }
constexpr int ulast(int u) { return u < 40 ? 8 * u + 7 : u == 40 ? NS1 - 1 : NS1 + 4 * (u - 41) + 3; }
constexpr int page(int u) { int r = u < 80 ? (u + 11) % RING : u - 80; return r < 15 ? r : r + 1; }
constexpr int prio(int r, int t) {
    int j = 0;
    while (PP[r][j + 1] <= t) j++;
    int e = -1;
    for (int g = 0; g < 4; g++)
        if (PP[0][j] <= TEP[g] && TEP[g] < PP[0][j + 1]) e = TEP[g];
    if (e >= 0) return r ? (t == PP[r][j] ? 2 : -1) : t == PP[r][j] ? 3 : t == e + 1 ? 0 : -1;
    int n = PP[r][j + 1] - PP[r][j], a = (t - PP[r][j]) * 4 / n;
    return t == PP[r][j] || a != (t - 1 - PP[r][j]) * 4 / n ? 3 - a : -1;
}
constexpr int NT = NS + LAG;
struct Sched {
    int nop;
    int op[200];
    int lo[NT + 1];
    int sync_at[NT];
    int sync_wait[NSYNC];
    int tok_wait[18], h_wait[18], poll_wait[4];
    int stat_wait, sd_wait, minlead;
    bool ok;
};
constexpr Sched make(int r) {
    Sched s{};
    s.ok = true;
    s.minlead = 1000;
    const int* P = PP[r];
    const int t0 = P[0];
    int est[320] = {}, need[320] = {}, pos[320] = {}, jv[NU] = {};
    bool live[320] = {};
    for (int u = 0; u < NU; u++) {
        int j = 0;
        while (ufirst(u) > PP[0][j + 1] + LK - 1) j++;
        jv[u] = j;
        need[u] = P[j];
        est[u] = u < G_UE ? P[j] - WU : u < 41 ? P[j] - G_WU2 : 0;
        if (u >= RING) {
            int jf = 0;
            while (PP[1][jf] <= ulast(u < 80 ? u - RING : u - 15)) jf++;
            if (jf >= j) s.ok = false;
            if (est[u] < P[jf]) est[u] = P[jf];
        }
        live[u] = true;
    }
    for (int k = 0; k < 36; k++) {
        need[100 + k] = (k >> 1) * 8;
        est[100 + k] = need[100 + k] - WT + (k & 1) * 4;
        need[200 + k] = hneed(k >> 1);
        int e = k < G_HE ? 0 : 290 + ((k - G_HE) & ~1), c = TPC[r][k < 16 ? 0 : k < 24 ? 1 : k < 32 ? 2 : 3];
        est[200 + k] = e > c ? e : c;
        live[100 + k] = live[200 + k] = true;
    }
    if (r == 1)
        for (int k = 300; k < 304; k++) { need[k] = TSTAT; est[k] = G_TSL; live[k] = true; }
    int n = 0, last = -1000, j = 0, pos_sd = 0, pos_st[4] = {}, pos_fl = 0;
    for (int t = t0 - 1; t < NS; t++) {
        if (t >= t0) {
            s.lo[t - t0] = s.nop;
            s.sync_at[t - t0] = -1;
            if (P[j] == t) {
                s.sync_at[t - t0] = j;
                int m = -1;
                for (int u = 0; u < NU; u++)
                    if (jv[u] <= j) { if (live[u]) s.ok = false; if (pos[u] > m) m = pos[u]; }
                if (r == 1 && j >= 6 && pos[300] > m) m = pos[300];
                int g = isflag(j);
                if (g >= 0 && pos_st[g] > m) m = pos_st[g];
                if (g >= 0 && t <= THS[g == 0 ? 3 : g == 3 ? 8 : 2 * g + 3]) s.ok = false;
                s.sync_wait[j] = n - 1 - m;
                j++;
            }
            for (int g = 0; g < 4; g++)
                if (t == TPC[r][g]) s.poll_wait[g] = n - 1 - pos_fl;
        }
        while (true) {
            int b = -1;
            for (int o = 0; o < 304; o++)
                if (live[o] && est[o] <= t && (b < 0 || need[o] < need[b])) b = o;
            if (b < 0) break;
            if (t >= 0 && need[b] - t > G_URG && 2 * t - last < G_PACE) break;
            live[b] = false;
            pos[b] = n++;
            s.op[s.nop++] = b;
            last = 2 * t - last < G_PACE + 2 && 2 * t - last >= G_PACE ? last + G_PACE : 2 * t;
            if (t >= 40 && need[b] - t < s.minlead) s.minlead = need[b] - t;
            if (t >= 0) break;
        }
        if (r == 1 && t == TSTAT) { if (live[303]) s.ok = false; s.stat_wait = n - 1 - pos[303]; n++; }
        if (t >= 0 && t < 144 && t % 8 == 0) {
            int a = 100 + t / 4;
            if (live[a] || live[a + 1]) s.ok = false;
            s.tok_wait[t / 8] = n - 1 - (pos[a] > pos[a + 1] ? pos[a] : pos[a + 1]);
        }
        for (int ks = 0; ks < 18; ks++)
            if (t == hneed(ks)) {
                int a = 200 + ks * 2;
                if (live[a] || live[a + 1]) s.ok = false;
                s.h_wait[ks] = n - 1 - (pos[a] > pos[a + 1] ? pos[a] : pos[a + 1]);
            }
        if (t == TSD) { n += 2; pos_sd = n - 1; }
        if (t == 143) s.sd_wait = n - 1 - pos_sd;
        for (int i = 0; i < 9; i++)
            if (t == THS[i]) pos_st[i < 4 ? 0 : i / 2 - 1] = n++;
        if (t >= TZ0 && t < TZ0 + 4 * TZS && (t - TZ0) % TZS == 0) n++;
        for (int g = 0; g < 4; g++)
            if (t == TPL[r][g]) pos_fl = n++;
        if (t == TX[0] + 2 || t == TX[0] + 4 || t == TX[1] + 3 || t == TX[2] + 3 || (t >= TMISC && t < TMISC + 5)) n++;
    }
    s.lo[NS - t0] = s.nop;
    if (j != NSYNC || TL < P[NSYNC - 1] || TZ0 + 3 * TZS >= 143) s.ok = false;
    return s;
}
template <int R> struct SS { static constexpr Sched s = make(R); };
typedef char schedule_is_valid[SS<0>::s.ok && SS<1>::s.ok ? 1 : -1];
constexpr int TAB_OFF = 15 * 8192, XA_OFF = 15 * 8192, XB_OFF = 18 * 8192, SMEM = 20 * 8192;

template <int N>
__device__ __forceinline__ void wait_vm_reg2(u4& a, u4& b) {
    asm volatile("s_waitcnt vmcnt(%2)" : "+v"(a), "+v"(b) : "n"(N > 63 ? 63 : N));
}
__device__ __forceinline__ float half_sum(float v) {
    v = row_sum(v);
    float w = v;
    asm volatile("s_nop 1\n\tv_permlane16_swap_b32 %0, %1\n\ts_nop 1" : "+v"(v), "+v"(w));
    return v + w;
}
typedef __attribute__((address_space(3))) f4* lds_f4w;
__device__ __forceinline__ void gload_sc1(u4& r, unsigned voff, const char* base) {
    asm volatile("global_load_dwordx4 %0, %1, %2 sc1" : "=v"(r) : "v"(voff), "s"(base));
}
__device__ __forceinline__ void st16(u4 o, unsigned voff, char* base) {
    asm volatile("global_store_dwordx4 %0, %1, %2 sc1\n\ts_nop 1" ::"v"(voff), "v"(o), "s"(base) : "memory");
}
template <int HI>
__device__ __forceinline__ void st4(unsigned o, unsigned voff, char* base) {
    asm volatile("s_mov_b32 exec_lo, 1\n\ts_mov_b32 exec_hi, %3\n\tglobal_store_dword %0, %1, %2 sc1\n\ts_mov_b64 exec, -1" ::"v"(voff), "v"(o), "s"(base), "n"(HI) : "memory");
}
__device__ __forceinline__ void ld4(unsigned& o, unsigned voff, const char* base) {
    asm volatile("global_load_dword %0, %1, %2 sc1" : "=v"(o) : "v"(voff), "s"(base));
}
__device__ __forceinline__ void pub(u4 o, unsigned voff, char* base) {
    o[0] = o[0] > 1u ? o[0] : 1u;
    o[2] = o[2] > 1u ? o[2] : 1u;
    st16(o, voff, base);
}
template <int O>
__device__ __forceinline__ void st16o(u4 o, unsigned voff, char* base) {
    asm volatile("global_store_dwordx4 %0, %1, %2 offset:%3 sc1\n\ts_nop 1" ::"v"(voff), "v"(o), "s"(base), "n"(O) : "memory");
}
__device__ __forceinline__ void swap2(u4& a, u4& b) {
    unsigned a2 = a[2], a3 = a[3], b0 = b[0], b1 = b[1];
    asm volatile("v_swap_b32 %0, %2\n\tv_swap_b32 %1, %3\n\ts_nop 1" : "+v"(a2), "+v"(a3), "+v"(b0), "+v"(b1));
    a[2] = a2; a[3] = a3; b[0] = b0; b[1] = b1;
}
template <int N>
__device__ __forceinline__ void poll(unsigned& fl, unsigned voff, const char* base, unsigned long long mask) {
    asm volatile(
        "s_waitcnt vmcnt(%4)\n"
        "1:\n\t"
        "v_cmp_eq_u32 vcc, 0, %0\n\t"
        "s_nop 1\n\t"
        "s_and_b64 vcc, vcc, %3\n\t"
        "s_cbranch_scc0 2f\n\t"
        "s_sleep 1\n\t"
        "global_load_dword %0, %1, %2 sc1\n\t"
        "s_waitcnt vmcnt(0)\n\t"
        "s_branch 1b\n"
        "2:"
        : "+v"(fl)
        : "v"(voff), "s"(base), "s"(mask), "n"(N > 63 ? 63 : N)
        : "vcc", "scc", "memory");
}

template <int R>
__device__ __forceinline__ void body(unsigned sm, char* ws, char* out, int wid, int lane, unsigned pf) {
    using S = SS<R>;
    constexpr int T0 = PP[R][0];
    const int b = blockIdx.x, xcd = b & 7, idx = b >> 3;
    const int mt = (xcd & 1) * 4 + (idx & 3), nt = (xcd >> 1) * 8 + (idx >> 2);
    const int p = xcd >> 1, mt2 = (xcd & 1) * 8 + (idx & 7), nt2 = idx >> 3;
    const int tk = (wid & 3) * 2 + R;
    unsigned lb0 = sm + lane * 16;
    asm volatile("" : "+v"(lb0));
    char* wb = ws - sm;
    const char* xsrc = ws + WS_X2O;
    const unsigned voff = ((lane >> 4) * 2048 + mt * 256 + wid * 32 + (lane & 15)) * 16;
    const char* wsrc = wb + WS_W1 + (size_t)nt * STREAM_B + wid * 1024;
    const char* wsrc2 = wb + WS_WD + (size_t)(nt2 * 4 + p) * STREAM_B + wid * 1024;
    char* hdst = wb + WS_H + ((size_t)((mt * 16 + wid * 2) * 144 + (nt >> 1) * 9) * 64) * 16 + (nt & 1) * 4096;
    char* hdst1 = hdst + 147456;
    const char* h0 = wb + WS_H + (size_t)((mt2 * 8 + tk) * 144 + p * 36 + R * 18) * 1024;
    const char* h1 = h0 + (R ? -147456 : 147456);
    char* sdp = ws + WS_SD + mt * 1024;
    char* part = wb + WS_PART + (size_t)(mt2 * 4 + nt2) * (128 * 1024);
    const char* flp = ws + WS_FLAG + (mt2 >> 1) * 512;
    const unsigned wdst = sm + wid * 1024;
    u4 tok[36], hk[36], st[3];
    auto issue1 = [&](auto I) {
        constexpr int op = S::s.op[decltype(I)::value];
        if constexpr (op < 41) glds16(wdst + page(op) * 8192, lb0, wsrc + op * 8192);
        else if constexpr (op < 100) glds16(wdst + page(op) * 8192, lb0, wsrc2 + (op - 41) * 8192);
        else if constexpr (op < 200) gload16(tok[op - 100], voff, xsrc + ((op - 100) >> 1) * 131072 + ((op - 100) & 1) * 256);
        else if constexpr (op < 300) gload_sc1(hk[op - 200], lb0, ((op & 1) ? h1 : h0) + g2_inv((op - 200) >> 1) * 1024);
        else if constexpr (op == 300) glds16(sm + TAB_OFF + (wid & 1) * 1024, lb0, wb + WS_U12 + nt * 2048 + (wid & 1) * 1024);
        else gload16(st[op - 301], (lane & (op == 303 ? 7 : 31)) * 32768 + (lane >> 5) * 16,
                     xsrc + (mt * 256 + nt * 8 + (wid & 3) * 2) * 16 + (op - 301) * 32 * 32768);
    };
    static_for<0, S::s.lo[0]>(issue1);
    unsigned lbc = 0;
    auto frag = [&](auto FI) -> u4 {
        constexpr int f = decltype(FI)::value;
        constexpr auto foff = [](int f) { return f < NS1 ? page(f / 8) * 8192 + (f % 8) * 1024 : page(41 + (f - NS1) / 4) * 8192 + ((f - NS1) % 4) * 2048 + R * 1024; };
        constexpr int o = foff(f);
        if constexpr (f == 0 || (foff(f - 1) >> 16) != (o >> 16)) {
            if constexpr (o >= 65536) { lbc = lb0 + (o & ~65535); asm volatile("" : "+v"(lbc)); }
        }
        return o < 65536 ? ldsr(lb0 + o) : ldsr(lbc + (o & 65535));
    };
    f4 acc[8][2], aco[2][2], xv[7], gb0, gb1;
    u4 fr[LK + 1], x2v, hs[4], pl[4];
    float rs[2] = {0.f, 0.f};
    unsigned sdv[2], fl, x2c, gbc, xw = 0, xr = 0, off8 = 0;
    int trow = 0;
    const char* gbp = ws + WS_MODF + 5 * D_ * 4;
    static_for<T0, NS>([&](auto TI) {
        constexpr int t = decltype(TI)::value;
        if constexpr (S::s.sync_at[t - T0] >= 0) {
            constexpr int j = S::s.sync_at[t - T0];
            __builtin_amdgcn_sched_barrier(0);
            if constexpr (t == TX[0] || t == TX[1] || t == TX[2]) asm volatile("s_waitcnt lgkmcnt(0)" ::: "memory");
            wait_vm<S::s.sync_wait[j]>();
            __builtin_amdgcn_s_barrier();
            asm volatile("" ::: "memory");
            if constexpr (j == 1) asm volatile("" ::"v"(pf));
            if constexpr (R == 0 && isflag(j) >= 0)
                if ((wid & 3) == 0) st4<0>(1u, 0u, ws + WS_FLAG + mt * 512 + nt * 4 + isflag(j) * 128);
            __builtin_amdgcn_sched_barrier(0);
        }
        if constexpr (G_PRIO && t >= 0 && prio(R, t) >= 0) __builtin_amdgcn_s_setprio(prio(R, t));
        if constexpr (t == TPC[R][0]) poll<S::s.poll_wait[0]>(fl, (lb0 - sm) >> 2, flp, 15ull << (p * 8 + R * 4));
        if constexpr (t == TPC[R][1]) poll<S::s.poll_wait[1]>(fl, (lb0 - sm) >> 2, flp, (15ull << (32 + p * 8 + R * 4)) | 0xffffffffull);
        if constexpr (t == TPC[R][2]) poll<S::s.poll_wait[2]>(fl, (lb0 - sm) >> 2, flp + 256, 15ull << (p * 8 + R * 4));
        if constexpr (t == TPC[R][3]) poll<S::s.poll_wait[3]>(fl, (lb0 - sm) >> 2, flp + 256, 15ull << (32 + p * 8 + R * 4));
        static_for<S::s.lo[t - T0], S::s.lo[t - T0 + 1]>(issue1);
        if constexpr (R == 1 && t == TSTAT) {
            asm volatile("s_waitcnt vmcnt(%3)" : "+v"(st[0]), "+v"(st[1]), "+v"(st[2]) : "n"(S::s.stat_wait));
            const bool x = (lane & 31) < 8;
            float v[24];
            for (int i = 0; i < 12; i++) {
                unsigned a = st[i >> 2][i & 3];
                if (i >= 8) a = x ? a : 0u;
                v[2 * i] = lo2f(a);
                v[2 * i + 1] = hi2f(a);
            }
            float a0 = 0.f, a1 = 0.f;
            for (int i = 0; i < 12; i++) { a0 += v[i]; a1 += v[12 + i]; }
            const float mean = half_sum(a0 + a1) * (1.f / D_);
            float q0 = 0.f, q1 = 0.f, q2 = 0.f;
            for (int i = 0; i < 8; i++) {
                float d0 = v[i] - mean, d1 = v[8 + i] - mean, d2 = v[16 + i] - mean;
                q0 += d0 * d0; q1 += d1 * d1; q2 += d2 * d2;
            }
            const float var = half_sum(q0 + q1 + (x ? q2 : 0.f)) * (1.f / D_);
            st4<1>(((unsigned)f2bf(__builtin_sqrtf(rbf(rbf(var) + 1e-5f))) << 16) | 1u, (lane >> 5) * 4, sdp + (nt * 8 + (wid & 3) * 2) * 4);
            __builtin_amdgcn_sched_barrier(0);
        }
        if constexpr (t >= 0) {
            if constexpr (t == 0) static_for<0, LK>([&](auto FI) { fr[decltype(FI)::value] = frag(FI); });
            constexpr int t2 = t - NS1;
            constexpr int ks = t < 144 ? t / 8 : t < 288 ? (t % 72) / 4 : t < NS1 ? (t - 288) / 2 : t2 < 84 ? t2 / 7 : t2 < 102 ? (t2 - 48) / 3 : t2 < 126 ? 12 + (t2 - 102) % 12 / 2 : (t2 - 126) / 2;
            constexpr int a = t < 144 ? t % 8 : t < 288 ? t % 4 : (t < NS1 || t2 >= 126) ? t % 2 : t2 < 84 ? t2 % 7 : t2 < 102 ? t2 % 3 : (t2 < 114 ? 3 : 5) + t % 2;
            if constexpr (t < 144 && a == 0) wait_vm_reg2<S::s.tok_wait[ks]>(tok[2 * ks], tok[2 * ks + 1]);
            if constexpr (t == hneed(ks)) {
                wait_vm_reg2<S::s.h_wait[ks]>(hk[2 * ks], hk[2 * ks + 1]);
                if constexpr (ks >= 16) swap2(hk[2 * ks + R], hk[2 * ks + 1 - R]);
            }
            if constexpr (t + LK < NS) fr[(t + LK) % (LK + 1)] = frag(std::integral_constant<int, t + LK>{});
            for (int sub = 0; sub < 2; sub++) {
                f4& c = t2 >= 126 ? aco[a & 1][sub] : acc[a][sub];
                const u4& bo = t2 >= 0 ? hk[2 * ks + sub] : tok[2 * ks + sub];
                if constexpr (ks == 0) mfma0(c, fr[t % (LK + 1)], bo);
                else mfma(c, fr[t % (LK + 1)], bo);
                __builtin_amdgcn_sched_barrier(0);
            }
            if constexpr (t == TSD) {
                unsigned o = (lb0 - sm) >> 2 & 0x3c;
                asm volatile("global_load_dword %0, %2, %3 sc1\n\tglobal_load_dword %1, %2, %3 offset:64 sc1"
                             : "=&v"(sdv[0]), "=&v"(sdv[1]) : "v"(o), "s"(sdp + wid * 128));
            }
            if constexpr (t == 143) {
                unsigned o = (lb0 - sm) >> 2 & 0x3c, tmp;
                asm volatile(
                    "s_waitcnt vmcnt(%5)\n"
                    "1:\n\t"
                    "v_and_b32 %2, %0, %1\n\t"
                    "v_and_b32 %2, 1, %2\n\t"
                    "v_cmp_eq_u32 vcc, 0, %2\n\t"
                    "s_cbranch_vccz 2f\n\t"
                    "s_sleep 2\n\t"
                    "global_load_dword %0, %3, %4 sc1\n\t"
                    "global_load_dword %1, %3, %4 offset:64 sc1\n\t"
                    "s_waitcnt vmcnt(0)\n\t"
                    "s_branch 1b\n"
                    "2:"
                    : "+v"(sdv[0]), "+v"(sdv[1]), "=&v"(tmp)
                    : "v"(o), "s"(sdp + wid * 128), "n"(S::s.sd_wait)
                    : "vcc");
                for (int sub = 0; sub < 2; sub++) {
                    unsigned bb = sdv[sub] & 0xffff0000u;
                    rs[sub] = __builtin_amdgcn_rcpf(__builtin_bit_cast(float, bb));
                }
            }
            if constexpr (t == TEP[0] || t == TEP[1] || t == TEP[2] || t == TEP[3]) {
                constexpr int g = t / 72 - 1;
                __builtin_amdgcn_sched_barrier(0);
                asm volatile("s_nop 7");
                constexpr int NC = g == 0 ? 4 : g < 3 ? 2 : 1, C0 = g == 0 ? 0 : 2 * g + 2;
                unsigned tb = sm + TAB_OFF + (((lb0 - sm) >> 8) << 4);
                asm volatile("" : "+v"(tb));
                f4 ta = ldsrf(tb + C0 * 128), tc = ldsrf(tb + C0 * 128 + 64);
                static_for<0, NC>([&](auto CI) {
                    constexpr int c = decltype(CI)::value;
                    f4 ua = ta, uc = tc;
                    if constexpr (c + 1 < NC) {
                        ta = ldsrf(tb + (C0 + c + 1) * 128);
                        tc = ldsrf(tb + (C0 + c + 1) * 128 + 64);
                    }
                    for (int sub = 0; sub < 2; sub++) {
                        f4 pr = (acc[2 * c][sub] * rs[sub] + ua) * (acc[2 * c + 1][sub] * rs[sub] + uc);
                        u4& o = hs[g < 3 ? (c & 2) + sub : 0];
                        const int h = g < 3 ? (c & 1) * 2 : sub * 2;
                        o[h] = pkbf(pr[0], pr[1]);
                        o[h + 1] = pkbf(pr[2], pr[3]);
                    }
                    __builtin_amdgcn_sched_barrier(0);
                });
            }
            static_for<0, 8>([&](auto II) {
                constexpr int i = decltype(II)::value;
                if constexpr (t == THS[i]) st16o<(i >> 1) * 1024>(hs[i < 4 ? i : i & 1], lb0, (i & 1) ? hdst1 : hdst);
            });
            if constexpr (t == THS[8]) st16(hs[0], lb0, hdst + 8192 + (nt & 1) * (147456 - 4096));
            if constexpr (t >= TZ0 && t < TZ0 + 4 * TZS && (t - TZ0) % TZS == 0)
                st16(u4{0, 0, 0, 0}, lb0, wb + WS_PART + (size_t)(mt * 32 + nt) * 32768 + ((t - TZ0) / TZS * 8 + wid) * 1024);
            if constexpr (t == TPL[R][0] || t == TPL[R][1]) ld4(fl, (lb0 - sm) >> 2, flp);
            if constexpr (t == TPL[R][2] || t == TPL[R][3]) ld4(fl, (lb0 - sm) >> 2, flp + 256);
            if constexpr (t == TX[0] - 3) {
                xw = lb0 + XA_OFF + wid * 1024;
                xr = lb0 + XA_OFF + (wid ^ 4) * 1024;
            }
            static_for<0, 7>([&](auto CI) {
                constexpr int c = decltype(CI)::value, g = c < 3 ? 0 : (c - 1) >> 1, i = c < 3 ? c : (c - 1) & 1;
                constexpr int o = (g == 1 ? XB_OFF - XA_OFF : 0) + i * 8192;
                if constexpr (t == TX[g] - (g ? 2 : 3) + i) *(lds_f4w)(size_t)(xw + o) = acc[c][1];
                if constexpr (t == TX[g] + i) xv[c] = ldsrf(xr + o);
                if constexpr (c == 0 && t == TX[0] + 2) pub(__builtin_bit_cast(u4, acc[0][0] + xv[0]), lb0, part + 96 * 1024 + (p * 8 + tk) * 1024);
                if constexpr ((c & 1) && t == TX[g] + (g ? 3 : 4)) {
                    const f4 va = acc[c][0] + xv[c], vb = acc[c + 1][0] + xv[c + 1];
                    pub(u4{pkbf(va[0], vb[0]), pkbf(va[1], vb[1]), pkbf(va[2], vb[2]), pkbf(va[3], vb[3])}, lb0,
                        part + ((((p + 1 + g) & 3) * 3 + g) * 8 + tk) * 1024);
                }
            });
            if constexpr (t == TMISC) {
                const int q = lane >> 4, tl = lane & 15;
                trow = (mt2 * 8 + tk) * 16 + tl;
                off8 = sm + 96 * 1024 + (q * 8 + tk) * 1024 + (p * 16 + tl) * 16;
                gload16(x2v, ((nt2 * 18 + p * 4 + q) * 2048 + trow) * 16, xsrc);
            }
            if constexpr (t == TMISC + 1)
                asm volatile("global_load_ushort %0, %1, %2" : "=v"(x2c) : "v"(((nt2 * 18 + 16 + (p >> 1)) * 2048 + trow) * 16 + ((p & 1) * 4 + (lane >> 4)) * 2), "s"(xsrc));
            if constexpr (t == TMISC + 2) asm volatile("global_load_dwordx4 %0, %1, %2" : "=v"(gb0) : "v"((lane >> 4) * 32), "s"(gbp + (nt2 * 144 + p * 32) * 4));
            if constexpr (t == TMISC + 3) asm volatile("global_load_dwordx4 %0, %1, %2 offset:16" : "=v"(gb1) : "v"((lane >> 4) * 32), "s"(gbp + (nt2 * 144 + p * 32) * 4));
            if constexpr (t == TMISC + 4) asm volatile("global_load_dword %0, %1, %2" : "=v"(gbc) : "v"((lane >> 4) * 4), "s"(gbp + (nt2 * 144 + 128 + p * 4) * 4));
            if constexpr (t == TL) gload_sc1(pl[3], off8, part);
            if constexpr (t > TL && t < TL + 4) gload_sc1(pl[t - TL - 1], lb0, part + ((p * 3 + t - TL - 1) * 8 + tk) * 1024);
            __builtin_amdgcn_sched_barrier(0);
        }
    });
    asm volatile("s_nop 7");
    *(lds_f4w)(size_t)(xw + XB_OFF - XA_OFF) = aco[0][1];
    *(lds_f4w)(size_t)(xw + XB_OFF - XA_OFF + 8192) = aco[1][1];
    asm volatile("s_waitcnt lgkmcnt(0)" ::: "memory");
    __builtin_amdgcn_s_barrier();
    asm volatile("" ::: "memory");
    f4 o0 = aco[0][0] + ldsrf(xr + XB_OFF - XA_OFF), o1 = aco[1][0] + ldsrf(xr + XB_OFF - XA_OFF + 8192);
    asm volatile("s_waitcnt vmcnt(0)" : "+v"(pl[0]), "+v"(pl[1]), "+v"(pl[2]), "+v"(pl[3]), "+v"(x2v), "+v"(gb0), "+v"(gb1), "+v"(x2c), "+v"(gbc));
    while (true) {
        unsigned tg = 1u;
        for (int i = 0; i < 4; i++) tg = min(tg, min(pl[i][0], pl[i][2]));
        if (__builtin_amdgcn_ballot_w64(tg == 0) == 0) break;
        __builtin_amdgcn_s_sleep(1);
        for (int i = 0; i < 3; i++) gload_sc1(pl[i], lb0, part + ((p * 3 + i) * 8 + tk) * 1024);
        gload_sc1(pl[3], off8, part);
        asm volatile("s_waitcnt vmcnt(0)" : "+v"(pl[0]), "+v"(pl[1]), "+v"(pl[2]), "+v"(pl[3]));
    }
    const int q = lane >> 4;
    for (int r = 0; r < 4; r++) {
        o0[r] += (lo2f(pl[0][r]) + lo2f(pl[1][r])) + lo2f(pl[2][r]);
        o1[r] += (hi2f(pl[0][r]) + hi2f(pl[1][r])) + hi2f(pl[2][r]);
    }
    u4 ov;
    for (int r = 0; r < 4; r++) {
        unsigned ob = pkbf(o0[r], o1[r]);
        float ga = r < 2 ? gb0[2 * r] : gb1[2 * r - 4], gc = r < 2 ? gb0[2 * r + 1] : gb1[2 * r - 3];
        unsigned pb = pkbf(ga * lo2f(ob), gc * hi2f(ob));
        ov[r] = pkbf(lo2f(x2v[r]) + lo2f(pb), hi2f(x2v[r]) + hi2f(pb));
    }
    *(u4*)(out + (size_t)trow * 1152 + (nt2 * 144 + p * 32 + q * 8) * 2) = ov;
    {
        const f4 p8 = __builtin_bit_cast(f4, pl[3]);
        float u0 = p8[0], u1 = p8[1], u2 = p8[2], u3 = p8[3];
        asm volatile("s_nop 1\n\tv_permlane16_swap_b32 %0, %1\n\tv_permlane16_swap_b32 %2, %3" : "+v"(u0), "+v"(u1), "+v"(u2), "+v"(u3));
        float a = u0 + u1, c = u2 + u3;
        asm volatile("s_nop 1\n\tv_permlane32_swap_b32 %0, %1" : "+v"(a), "+v"(c));
        float o = a + c;
        *(u16*)(out + (size_t)trow * 1152 + (nt2 * 144 + 128 + p * 4 + q) * 2) = f2bf(lo2f(x2c) + rbf(__builtin_bit_cast(float, gbc) * rbf(o)));
    }
    if constexpr (R) asm volatile(".Lpfe1:");
    else asm volatile(".Lpfe0:");
}
}

__global__ NOPK __launch_bounds__(512, 2) void k_g(char* ws, char* out) {
    extern __shared__ __attribute__((aligned(16))) char smem[];
    const unsigned sm = lds_addr(smem);
    const int tid = threadIdx.x, lane = tid & 63;
    const int wid = __builtin_amdgcn_readfirstlane(tid >> 6);
    const unsigned pf = PFG ? code_pf(code_range(), wid, lane) : 0u;
    if ((wid < 4) != G_LEADHI) {
        gf::body<0>(sm, ws, out, wid, lane, pf);
        __builtin_amdgcn_endpgm();
    }
    gf::body<1>(sm, ws, out, wid, lane, pf);
}

extern "C" void kernel_launch(void* const* d_in, const int* in_sizes, int n_in,
                              void* d_out, int out_size, void* d_ws, size_t ws_size,
                              hipStream_t stream) {
    char* ws = (char*)d_ws;
    AArgs a;
    a.x = (const u16*)d_in[0];
    a.Are = (const u16*)d_in[1];
    a.Aim = (const u16*)d_in[2];
    a.Bre = (const u16*)d_in[3];
    a.Bim = (const u16*)d_in[4];
    a.Cre = (const u16*)d_in[5];
    a.Cim = (const u16*)d_in[6];
    a.logdt = (const u16*)d_in[7];
    a.c = (const u16*)d_in[13];
    a.wmod = (const u16*)d_in[14];
    a.wd = (const u16*)d_in[12];
    a.ws = ws;
    BArgs bb;
    bb.dskip = (const u16*)d_in[8];
    bb.wg = (const u16*)d_in[10];
    bb.wu = (const u16*)d_in[11];
    bb.ws = ws;
    (void)hipFuncSetAttribute((const void*)k_b, hipFuncAttributeMaxDynamicSharedMemorySize, B_SMEM);
    (void)hipFuncSetAttribute((const void*)k_g, hipFuncAttributeMaxDynamicSharedMemorySize, gf::SMEM);
    k_a<<<A_KG + A_LN + A_MOD + A_WD + A_ZERO, 256, 0, stream>>>(a);
    k_b<<<B_PF + B_CONV + B_PREP, 512, B_SMEM, stream>>>(bb);
    k_g<<<256, 512, gf::SMEM, stream>>>(ws, (char*)d_out);
}
```
